```python
import torch

def _softmax(x, axis=-1):
    return torch.softmax(x, dim=axis)

def _gelu(x):
    return torch.nn.functional.gelu(x, approximate='tanh')

def _silu(x):
    return torch.nn.functional.silu(x)

def _sigmoid(x):
    return torch.sigmoid(x)

def _layernorm(x, axis=-1, eps=1e-05):
    m = x.mean(dim=axis, keepdim=True)
    v = x.var(dim=axis, keepdim=True, unbiased=False)
    return (x - m) / torch.sqrt(v + eps)

def _scatter_add(tgt, idx, src, axis=0):
    return tgt.clone().index_add_(axis, idx, src)

def _segment_sum(src, seg, n):
    out = torch.zeros((n,) + src.shape[1:], dtype=src.dtype)
    return out.index_add_(0, seg, src)

def _pad(x, pw):
    flat = tuple((v for pair in reversed(pw) for v in pair))
    return torch.nn.functional.pad(x, flat)
kernel_name = 'adaLN_cond__sliding_block_cached__mixer_token_mlp__decode'

def setup_inputs():

    _w = lambda a: (lambda t: t.to(torch.bfloat16) if t.is_floating_point() else t)(torch.as_tensor(a))

    def _A_setup():
        B, Sq, D = (1, 1, 576)
        Hq, Hkv, Sk, Dh = (32, 16, 8192, 128)
        assert Hq % Hkv == 0, (Hq, Hkv)
        return {'x': _w((lambda *s: torch.randn(*s, dtype=torch.bfloat16) * 0.05)(B, Sq, D)), 'k_cache': _w((lambda *s: torch.randn(*s, dtype=torch.bfloat16) * 1.6)(B, Hkv, Sk, Dh)), 'v_cache': _w((lambda *s: torch.randn(*s, dtype=torch.bfloat16) * 1.6)(B, Hkv, Sk, Dh)), 'wq': _w((lambda *s: torch.randn(*s, dtype=torch.bfloat16) * 1.6)(D, Hq * Dh) * D ** (-0.5)), 'wo': _w((lambda *s: torch.randn(*s, dtype=torch.bfloat16) * 1.6)(Hq * Dh, D) * (Hq * Dh) ** (-0.5))}
    _A_in = _A_setup()

    def _B_setup():
        B, S, D, F = (1, 1, 576, 4608)
        return {'x': _w((lambda *s: torch.randn(*s, dtype=torch.bfloat16) * 0.05)(B, S, D)), 'w1': _w((lambda *s: torch.randn(*s, dtype=torch.bfloat16) * 1.6)(S, F) * S ** (-0.5)), 'b1': _w((lambda *s: torch.randn(*s, dtype=torch.bfloat16) * 1.6)(F) * 0.02), 'w2': _w((lambda *s: torch.randn(*s, dtype=torch.bfloat16) * 1.6)(F, S) * F ** (-0.5)), 'b2': _w((lambda *s: torch.randn(*s, dtype=torch.bfloat16) * 1.6)(S) * 0.02)}
    _B_in = _B_setup()
    return {'A_x': _A_in['x'], 'A_k_cache': _A_in['k_cache'], 'A_v_cache': _A_in['v_cache'], 'A_wq': _A_in['wq'], 'A_wo': _A_in['wo'], 'B_x': _B_in['x'], 'B_w1': _B_in['w1'], 'B_b1': _B_in['b1'], 'B_w2': _B_in['w2'], 'B_b2': _B_in['b2'], 'c': _w((lambda *s: torch.randn(*s, dtype=torch.bfloat16) * 0.05)(1, 576) * 8.0), 'w_mod': _w((lambda *s: torch.randn(*s, dtype=torch.bfloat16) * 0.05)(576, 6 * 576) * 576 ** (-0.5) * 8.0)}

def reference(A_x, A_k_cache, A_v_cache, A_wq, A_wo, B_x, B_w1, B_b1, B_w2, B_b2, c, w_mod):

    def _A_ref(x, k_cache, v_cache, wq, wo):
        B, Sq, D = x.shape
        Hq, Dh = (32, 128)
        Hkv, Sk = (k_cache.shape[1], k_cache.shape[2])
        W = min(4096, max(256, Sk // 4))
        rep = Hq // Hkv
        q = (lambda x, *d: x.permute(*d))((x @ wq).reshape(B, Sq, Hq, Dh), 0, 2, 1, 3)
        k = (lambda x, n, axis: torch.repeat_interleave(x, n, dim=axis))(k_cache[:, :, Sk - W:, :], rep, axis=1)
        v = (lambda x, n, axis: torch.repeat_interleave(x, n, dim=axis))(v_cache[:, :, Sk - W:, :], rep, axis=1)
        scores = torch.einsum('bhqd,bhkd->bhqk', q, k) * Dh ** (-0.5)
        p = _softmax(scores, axis=-1)
        o = torch.einsum('bhqk,bhkd->bhqd', p, v)
        return (lambda x, *d: x.permute(*d))(o, 0, 2, 1, 3).reshape(B, Sq, Hq * Dh) @ wo

    def _B_ref(x, w1, b1, w2, b2):
        B, S, D = x.shape
        h = _layernorm(x, axis=-1)
        u = torch.einsum('bsd,sf->bdf', h, w1) + b1
        u = _gelu(u)
        y = torch.einsum('bdf,fs->bds', u, w2) + b2
        return (lambda x, *d: x.permute(*d))(y, 0, 2, 1)
    x = A_x
    mod = (_silu(c) @ w_mod).reshape(c.shape[0], 6, 1, -1)
    sa, ca, ga, sb, cb, gb = (mod[:, 0].clone(), mod[:, 1].clone(), mod[:, 2].clone(), mod[:, 3].clone(), mod[:, 4].clone(), mod[:, 5].clone())
    h = _layernorm(x, axis=-1) * (1 + ca) + sa
    x = x + ga * _A_ref(h, A_k_cache, A_v_cache, A_wq, A_wo)
    h = _layernorm(x, axis=-1) * (1 + cb) + sb
    x = x + gb * _B_ref(h, B_w1, B_b1, B_w2, B_b2)
    return x
```

```cpp
#include <hip/hip_runtime.h>
#include <hip/hip_bf16.h>
#include <stdint.h>

typedef unsigned short u16;
__device__ __forceinline__ float bf2f(u16 x) { return __uint_as_float(((unsigned)x) << 16); }
__device__ __forceinline__ u16 f2bf(float f) { __hip_bfloat16 b = __float2bfloat16(f); return *reinterpret_cast<u16*>(&b); }
__device__ __forceinline__ float rbf(float f) { return bf2f(f2bf(f)); }

constexpr int D = 576, HQ = 32, HKV = 16, SK = 8192, DH = 128, W = 2048, F = 4608;
constexpr int NS = 16;

__device__ float block_sum(float v, float* red) {
  for (int o = 32; o >= 1; o >>= 1) v += __shfl_xor(v, o);
  int w = threadIdx.x >> 6, nw = blockDim.x >> 6;
  __syncthreads();
  if ((threadIdx.x & 63) == 0) red[w] = v;
  __syncthreads();
  float s = 0; for (int i = 0; i < nw; ++i) s += red[i];
  return s;
}

__global__ void k_mod(const u16* c, const u16* wmod, float* mod) {
  __shared__ float s[D]; __shared__ float part[4][64];
  for (int i = threadIdx.x; i < D; i += 256) { float x = bf2f(c[i]); s[i] = rbf(x / (1.f + __expf(-x))); }
  __syncthreads();
  int col = blockIdx.x * 64 + (threadIdx.x & 63), g = threadIdx.x >> 6;
  float acc = 0;
  for (int j = g; j < D; j += 4) acc += s[j] * bf2f(wmod[(size_t)j * 3456 + col]);
  part[g][threadIdx.x & 63] = acc;
  __syncthreads();
  if (threadIdx.x < 64) mod[col] = rbf(part[0][threadIdx.x] + part[1][threadIdx.x] + part[2][threadIdx.x] + part[3][threadIdx.x]);
}

__device__ void block_ln(float* v, float* red) {
  float s = 0; for (int i = threadIdx.x; i < D; i += blockDim.x) s += v[i];
  float mean = block_sum(s, red) / D;
  float q = 0; for (int i = threadIdx.x; i < D; i += blockDim.x) { float d = v[i] - mean; q += d * d; }
  float var = block_sum(q, red) / D;
  float mb = rbf(mean), vb = rbf(var);
  float sq = rbf(sqrtf(rbf(vb + 1e-5f)));
  __syncthreads();
  for (int i = threadIdx.x; i < D; i += blockDim.x) v[i] = rbf(rbf(v[i] - mb) / sq);
  __syncthreads();
}

__global__ void k_q(const u16* x, const float* mod, const u16* wq, float* q) {
  __shared__ float h[D]; __shared__ float red[8]; __shared__ float part[4][64];
  for (int i = threadIdx.x; i < D; i += 256) h[i] = bf2f(x[i]);
  __syncthreads();
  block_ln(h, red);
  for (int i = threadIdx.x; i < D; i += 256) h[i] = rbf(rbf(h[i] * rbf(1.f + mod[D + i])) + mod[i]);
  __syncthreads();
  int col = blockIdx.x * 64 + (threadIdx.x & 63), g = threadIdx.x >> 6;
  float acc = 0;
  for (int j = g; j < D; j += 4) acc += h[j] * bf2f(wq[(size_t)j * 4096 + col]);
  part[g][threadIdx.x & 63] = acc;
  __syncthreads();
  if (threadIdx.x < 64) q[col] = rbf(part[0][threadIdx.x] + part[1][threadIdx.x] + part[2][threadIdx.x] + part[3][threadIdx.x]);
}

__global__ void k_attn(const float* q, const u16* K, const u16* V, float* po, float* pml) {
  __shared__ float qs[DH]; __shared__ float p[128]; __shared__ float red[8];
  int s = blockIdx.x, h = blockIdx.y, kvh = h >> 1;
  qs[threadIdx.x] = q[h * DH + threadIdx.x];
  __syncthreads();
  int key = SK - W + s * 128 + threadIdx.x;
  const u16* kr = K + ((size_t)kvh * SK + key) * DH;
  float acc = 0; for (int d = 0; d < DH; ++d) acc += qs[d] * bf2f(kr[d]);
  float sc = rbf(rbf(acc) * 0.08838834764831845f);
  float m = sc; for (int o = 32; o >= 1; o >>= 1) m = fmaxf(m, __shfl_xor(m, o));
  if ((threadIdx.x & 63) == 0) red[threadIdx.x >> 6] = m;
  __syncthreads();
  m = fmaxf(red[0], red[1]);
  float e = __expf(sc - m);
  p[threadIdx.x] = e;
  float l = block_sum(e, red);
  __syncthreads();
  const u16* vb = V + ((size_t)kvh * SK + SK - W + s * 128) * DH;
  float o = 0; for (int k = 0; k < 128; ++k) o += p[k] * bf2f(vb[(size_t)k * DH + threadIdx.x]);
  po[((size_t)h * NS + s) * DH + threadIdx.x] = o;
  if (threadIdx.x == 0) { pml[(h * NS + s) * 2] = m; pml[(h * NS + s) * 2 + 1] = l; }
}

__global__ void k_wo(const float* po, const float* pml, const u16* wo, float* part) {
  __shared__ float o[DH];
  int h = blockIdx.x;
  if (threadIdx.x < DH) {
    float M = -INFINITY; for (int s = 0; s < NS; ++s) M = fmaxf(M, pml[(h * NS + s) * 2]);
    float L = 0, a = 0;
    for (int s = 0; s < NS; ++s) { float w = __expf(pml[(h * NS + s) * 2] - M); L += w * pml[(h * NS + s) * 2 + 1]; a += w * po[((size_t)h * NS + s) * DH + threadIdx.x]; }
    o[threadIdx.x] = rbf(a / L);
  }
  __syncthreads();
  for (int n = threadIdx.x; n < D; n += 256) {
    float acc = 0; for (int d = 0; d < DH; ++d) acc += o[d] * bf2f(wo[(size_t)(h * DH + d) * D + n]);
    part[h * D + n] = acc;
  }
}

__global__ void k_final(const u16* x, const float* mod, const float* part, const u16* w1, const u16* b1, const u16* w2, const u16* b2, u16* out) {
  __shared__ float x1[D]; __shared__ float h[D]; __shared__ float red[8];
  for (int i = threadIdx.x; i < D; i += 256) {
    float a = 0; for (int hh = 0; hh < HQ; ++hh) a += part[hh * D + i];
    float v = rbf(bf2f(x[i]) + rbf(mod[2 * D + i] * rbf(a)));
    x1[i] = v; h[i] = v;
  }
  __syncthreads();
  block_ln(h, red);
  for (int i = threadIdx.x; i < D; i += 256) h[i] = rbf(rbf(h[i] * rbf(1.f + mod[4 * D + i])) + mod[3 * D + i]);
  __syncthreads();
  block_ln(h, red);
  int d = blockIdx.x;
  float t = h[d];
  float acc = 0;
  for (int f = threadIdx.x; f < F; f += 256) {
    float u = t * bf2f(w1[f]) + bf2f(b1[f]);
    float z = 0.7978845608028654f * (u + 0.044715f * u * u * u);
    float g = 0.5f * u * (1.f + tanhf(z));
    acc += g * bf2f(w2[f]);
  }
  float y = block_sum(acc, red);
  if (threadIdx.x == 0) {
    y = rbf(rbf(y) + bf2f(b2[0]));
    out[d] = f2bf(x1[d] + rbf(mod[5 * D + d] * y));
  }
}

extern "C" void kernel_launch(void* const* d_in, const int* in_sizes, int n_in, void* d_out, int out_size, void* d_ws, size_t ws_size, hipStream_t stream) {
  const u16* x = (const u16*)d_in[0]; const u16* K = (const u16*)d_in[1]; const u16* V = (const u16*)d_in[2];
  const u16* wq = (const u16*)d_in[3]; const u16* wo = (const u16*)d_in[4];
  const u16* w1 = (const u16*)d_in[6]; const u16* b1 = (const u16*)d_in[7]; const u16* w2 = (const u16*)d_in[8]; const u16* b2 = (const u16*)d_in[9];
  const u16* c = (const u16*)d_in[10]; const u16* wmod = (const u16*)d_in[11];
  float* ws = (float*)d_ws;
  float* mod = ws; float* q = ws + 4096; float* po = ws + 8192; float* pml = po + HQ * NS * DH; float* part = pml + HQ * NS * 2;
  k_mod<<<54, 256, 0, stream>>>(c, wmod, mod);
  k_q<<<64, 256, 0, stream>>>(x, mod, wq, q);
  k_attn<<<dim3(NS, HQ), 128, 0, stream>>>(q, K, V, po, pml);
  k_wo<<<HQ, 256, 0, stream>>>(po, pml, wo, part);
  k_final<<<D, 256, 0, stream>>>(x, mod, part, w1, b1, w2, b2, (u16*)d_out);
}
```

```cpp
#include <hip/hip_runtime.h>
#include <stdint.h>

typedef unsigned short u16;
typedef unsigned long long u64;
typedef unsigned v4u __attribute__((ext_vector_type(4)));
typedef unsigned v2u __attribute__((ext_vector_type(2)));
typedef float f4 __attribute__((ext_vector_type(4)));
typedef float f2 __attribute__((ext_vector_type(2)));
typedef __bf16 bf2 __attribute__((ext_vector_type(2)));
typedef __bf16 bf8 __attribute__((ext_vector_type(8)));
#define GAS __attribute__((address_space(1)))
#define LAS __attribute__((address_space(3)))

constexpr int D = 576, NMOD = 6 * D, SK = 8192, DH = 128, W = 2048, F = 4608, NQ = 4096;
constexpr int NWG = 256, NTHR = 576;
constexpr int WS_EP = 0;
constexpr int WS_HG = 256;
constexpr int WS_QG = WS_HG + 4 * 288 * 16;
constexpr int WS_MG = WS_QG + 1024 * 16;
constexpr int WS_ML = WS_MG + 576 * 16;
constexpr int WS_AP = WS_ML + 2048;
constexpr int WS_PO = WS_AP + 9 * 16 * 256;
constexpr int WS_END = WS_PO + 16 * 8 * 1024;
constexpr unsigned SENT = 0xFFFFFFFFu;
constexpr int SPIN_CAP = 1 << 17;
constexpr int AUX_SC1 = 16;

#define STAMP(i) do { } while (0)
#define STAMP_DECL
#define FLUSH() do { } while (0)

struct Params {
  const u16 *x, *kc, *vc, *wq, *wo, *w1, *b1, *w2, *b2, *c, *wm;
  u16* out;
  unsigned char* ws;
};

constexpr int L_K = 0;
constexpr int L_RED = 65536;
constexpr int L_HT = L_RED + 2048;
constexpr int L_QS = L_HT + 2048;
constexpr int L_WM = L_QS + 512;
constexpr int L_WL = L_WM + 64;
constexpr int L_OW = L_WL + 64;
constexpr int L_ST0 = L_OW + 8192;
constexpr int L_ST1 = L_ST0 + 320;
constexpr int L_OWN = L_ST1 + 320;
constexpr int L_FIN = L_OWN + 64;
constexpr int L_TAG = L_FIN + 128;
constexpr int L_END = L_TAG + 16;
constexpr int L_BYTES = 96 * 1024;
static_assert(L_END <= L_BYTES, "lds");

__device__ __forceinline__ float bf2f(unsigned h) { return __uint_as_float(h << 16); }
__device__ __forceinline__ float rb(float x) { f2 v = {0.f, x}; return __builtin_bit_cast(float, __builtin_convertvector(v, bf2)); }
__device__ __forceinline__ unsigned pk2(float lo, float hi) { f2 v = {lo, hi}; return __builtin_bit_cast(unsigned, __builtin_convertvector(v, bf2)); }
__device__ __forceinline__ float dot2(unsigned a, unsigned b, float c) { return __builtin_amdgcn_fdot2_f32_bf16(__builtin_bit_cast(bf2, a), __builtin_bit_cast(bf2, b), c, false); }
template <int C> __device__ __forceinline__ unsigned dppu(unsigned x) { return (unsigned)__builtin_amdgcn_mov_dpp((int)x, C, 0xf, 0xf, true); }
template <int C> __device__ __forceinline__ float dpp(float x) { return __uint_as_float(dppu<C>(__float_as_uint(x))); }
constexpr int X1 = 0xB1, X2 = 0x4E, X7 = 0x141, R4 = 0x124, R8 = 0x128;
__device__ __forceinline__ float sw32(float a, float b) { auto r = __builtin_amdgcn_permlane32_swap(__float_as_uint(a), __float_as_uint(b), false, false); return __uint_as_float(r[0]) + __uint_as_float(r[1]); }
__device__ __forceinline__ float sw16(float a, float b) { auto r = __builtin_amdgcn_permlane16_swap(__float_as_uint(a), __float_as_uint(b), false, false); return __uint_as_float(r[0]) + __uint_as_float(r[1]); }
__device__ __forceinline__ float mx32(float a) { auto r = __builtin_amdgcn_permlane32_swap(__float_as_uint(a), __float_as_uint(a), false, false); return fmaxf(__uint_as_float(r[0]), __uint_as_float(r[1])); }
__device__ __forceinline__ float mx16(float a) { auto r = __builtin_amdgcn_permlane16_swap(__float_as_uint(a), __float_as_uint(a), false, false); return fmaxf(__uint_as_float(r[0]), __uint_as_float(r[1])); }
__device__ __forceinline__ float red8_hi(const float (&a)[8], int lane) {
  float v0 = sw32(a[0], a[4]), v1 = sw32(a[1], a[5]), v2 = sw32(a[2], a[6]), v3 = sw32(a[3], a[7]);
  float u0 = sw16(v0, v2), u1 = sw16(v1, v3);
  float t0 = u0 + dpp<R8>(u0), t1 = u1 + dpp<R8>(u1);
  return (lane & 8) ? t1 : t0;
}
__device__ __forceinline__ float sum_lo3(float v) { v += dpp<X1>(v); v += dpp<X2>(v); v += dpp<X7>(v); return v; }
__device__ __forceinline__ float max_lo3(float v) { v = fmaxf(v, dpp<X1>(v)); v = fmaxf(v, dpp<X2>(v)); v = fmaxf(v, dpp<X7>(v)); return v; }
__device__ __forceinline__ unsigned nosent(float v) { unsigned u = __float_as_uint(v); return u == SENT ? 0x7FC00000u : u; }
__device__ __forceinline__ v4u ldnt(const void* p) { return __builtin_nontemporal_load((const GAS v4u*)p); }
__device__ __forceinline__ v4u ldg(const void* p) { return *(const GAS v4u*)p; }
__device__ __forceinline__ unsigned ldh(const u16* p) { return *(const GAS u16*)p; }
#define BAR() do { asm volatile("s_waitcnt lgkmcnt(0)" ::: "memory"); __builtin_amdgcn_s_barrier(); asm volatile("" ::: "memory"); } while (0)
#define PIN() __builtin_amdgcn_sched_barrier(0)
typedef __amdgpu_buffer_rsrc_t rsrc_t;
__device__ __forceinline__ v4u ld16(rsrc_t r, int off) { return __builtin_amdgcn_raw_buffer_load_b128(r, off, 0, AUX_SC1); }
__device__ __forceinline__ v2u ld8(rsrc_t r, int off) { return __builtin_amdgcn_raw_buffer_load_b64(r, off, 0, AUX_SC1); }
__device__ __forceinline__ void st16(rsrc_t r, int off, v4u v) { __builtin_amdgcn_raw_buffer_store_b128(v, r, off, 0, AUX_SC1); }
__device__ __forceinline__ void st8(rsrc_t r, int off, v2u v) { __builtin_amdgcn_raw_buffer_store_b64(v, r, off, 0, AUX_SC1); }
__device__ __forceinline__ void st4(rsrc_t r, int off, unsigned v) { __builtin_amdgcn_raw_buffer_store_b32(v, r, off, 0, AUX_SC1); }
__device__ __forceinline__ v2u make_tag(v2u e) {
  u64 z = (((u64)e.y << 32) | e.x) + 0x9E3779B97F4A7C15ull;
  z = (z ^ (z >> 30)) * 0xBF58476D1CE4E5B9ull; z = (z ^ (z >> 27)) * 0x94D049BB133111EBull; z ^= z >> 31;
  v2u t = {(unsigned)z, (unsigned)(z >> 32)};
  t.x = __builtin_amdgcn_readfirstlane(t.x); t.y = __builtin_amdgcn_readfirstlane(t.y);
  return t;
}
__device__ __forceinline__ v4u poll_tag(rsrc_t r, int off, v2u tag) {
  v4u g;
  for (int s = 0; s < SPIN_CAP; ++s) {
    asm volatile("" ::: "memory");
    g = ld16(r, off);
    if (__all(g.y == tag.x && g.w == tag.y)) break;
  }
  return g;
}

__device__ __forceinline__ void row_fma(float (&acc)[8], v4u w, unsigned slo) {
  const unsigned shi = slo << 16;
#pragma unroll
  for (int k = 0; k < 4; ++k) { acc[2 * k] = dot2(w[k], slo, acc[2 * k]); acc[2 * k + 1] = dot2(w[k], shi, acc[2 * k + 1]); }
}
__device__ __forceinline__ void pair_rows(v4u& a, v4u& b) {
#pragma unroll
  for (int k = 0; k < 4; ++k) { const unsigned lo = __builtin_amdgcn_perm(b[k], a[k], 0x05040100u), hi = __builtin_amdgcn_perm(b[k], a[k], 0x07060302u); a[k] = lo; b[k] = hi; }
}
__device__ __forceinline__ void pair_fma(float (&acc)[8], v4u lo, v4u hi, unsigned s2) {
#pragma unroll
  for (int k = 0; k < 4; ++k) { acc[2 * k] = dot2(lo[k], s2, acc[2 * k]); acc[2 * k + 1] = dot2(hi[k], s2, acc[2 * k + 1]); }
}
__device__ __forceinline__ unsigned silu_bits(unsigned cb) {
  const float c = bf2f(cb);
  return __float_as_uint(rb(c / (1.f + __expf(-c)))) >> 16;
}

__device__ __forceinline__ float ln_input(LAS unsigned char* L, float xv, int wave, int lane) {
  float v = sw32(xv, xv * xv); v += dpp<R8>(v); v = sw16(v, v); v = sum_lo3(v);
  if ((lane & 31) == 0) ((LAS float*)(L + L_ST0))[wave * 8 + (lane >> 5)] = v;
  BAR();
  float s1 = 0.f, s2 = 0.f;
#pragma unroll
  for (int w = 0; w < 9; ++w) { const f2 u = *(LAS f2*)(L + L_ST0 + w * 32); s1 += u.x; s2 += u.y; }
  const float mean = s1 * (1.f / D), var = fmaxf(s2 * (1.f / D) - mean * mean, 0.f);
  const float sq = rb(__builtin_sqrtf(rb(rb(var) + 1e-5f)));
  return rb(rb(xv - rb(mean)) / sq);
}

__device__ __forceinline__ void o_stage(rsrc_t rs, LAS unsigned char* L, v4u (&wv)[4], int kvh, int wave, int lane) {
  pair_rows(wv[0], wv[1]); pair_rows(wv[2], wv[3]);
  const int hd = wave >> 2, sp = lane & 7;
  const int offo = WS_PO + ((kvh * 8 + sp) * 2 + hd) * 512 + (wave & 3) * 128 + (lane >> 3) * 16;
  const int offm = WS_ML + ((kvh * 2 + hd) * 8 + sp) * 8;
  v4u po; v2u ml;
  for (int s = 0; s < SPIN_CAP; ++s) {
    asm volatile("" ::: "memory");
    po = ld16(rs, offo); ml = ld8(rs, offm);
    if (__all(po.x != SENT && po.y != SENT && po.z != SENT && po.w != SENT && ml.x != SENT && ml.y != SENT)) break;
  }
  const float m = __uint_as_float(ml.x), M = max_lo3(m);
  const float e = __builtin_amdgcn_exp2f((m - M) * 1.4426950408889634f);
  const float Ls = sum_lo3(e * __uint_as_float(ml.y));
  const float wgt = e / Ls;
  float o[4];
#pragma unroll
  for (int k = 0; k < 4; ++k) o[k] = sum_lo3(wgt * __uint_as_float(po[k]));
  float acc[8] = {0, 0, 0, 0, 0, 0, 0, 0};
  pair_fma(acc, wv[0], wv[1], pk2(o[0], o[1])); pair_fma(acc, wv[2], wv[3], pk2(o[2], o[3]));
  const float r = red8_hi(acc, lane);
  ((LAS float*)(L + L_RED))[wave * 64 + 8 * (lane & 7) + (lane >> 3)] = r;
}
__device__ __forceinline__ void load_wo(const Params& p, v4u (&wv)[4], int kvh, int cb, int wave, int lane) {
  const u16* src = p.wo + (size_t)((2 * kvh + (wave >> 2)) * DH + 32 * (wave & 3) + 4 * (lane >> 3)) * D + 64 * cb + 8 * (lane & 7);
#pragma unroll
  for (int i = 0; i < 4; ++i) wv[i] = ldnt(src + i * D);
}
__device__ __forceinline__ void publish_ap(rsrc_t rs, LAS unsigned char* L, int kvh, int cb, int lane) {
  float s = 0.f;
#pragma unroll
  for (int w = 0; w < 8; ++w) s += ((LAS float*)(L + L_RED))[w * 64 + lane];
  st4(rs, WS_AP + (cb * 16 + kvh) * 256 + lane * 4, nosent(s));
}

__global__ void __launch_bounds__(NTHR) fused_k(Params p) {
  __shared__ __attribute__((aligned(16))) unsigned char smem[L_BYTES];
  LAS unsigned char* L = (LAS unsigned char*)smem;
  const int lane = threadIdx.x & 63, wave = __builtin_amdgcn_readfirstlane(threadIdx.x >> 6);
  const int xc = blockIdx.x & 7, j = blockIdx.x >> 3;
  const bool helper = wave == 8;
  const rsrc_t rs = __builtin_amdgcn_make_buffer_rsrc((void*)p.ws, 0, WS_END, 0x00020000);
  STAMP_DECL;
  STAMP(0);
  int fi;
  v2u tag;
  if (j < 5) {
    const int u = xc + 8 * j;
    if (u >= 36) return;
    const int g = lane >> 3, ch = lane & 7;
    if (!helper) {
      const int cb = u % 18, r0 = 288 * (u / 18) + 36 * wave + g;
      unsigned cv[5]; v4u wv[5];
#pragma unroll
      for (int i = 0; i < 4; ++i) cv[i] = ldh(p.c + r0 + 8 * i);
      cv[4] = ldh(p.c + r0 + (g < 4 ? 32 : 0));
      const u16* src = p.wm + (size_t)r0 * NMOD + 64 * cb + 8 * ch;
#pragma unroll
      for (int i = 0; i < 4; ++i) wv[i] = ldnt(src + (size_t)8 * i * NMOD);
      { const rsrc_t rw = __builtin_amdgcn_make_buffer_rsrc((void*)p.wm, 0, D * NMOD * 2, 0x00020000);
        wv[4] = __builtin_amdgcn_raw_buffer_load_b128(rw, g < 4 ? ((r0 + 32) * NMOD + 64 * cb + 8 * ch) * 2 : 0x7FFFFFF0, 0, 2); }
      PIN();
      float acc[8] = {0, 0, 0, 0, 0, 0, 0, 0};
#pragma unroll
      for (int i = 0; i < 5; ++i) cv[i] = silu_bits(cv[i]);
#pragma unroll
      for (int i = 0; i < 5; ++i) row_fma(acc, wv[i], cv[i]);
      STAMP(1);
      ((LAS float*)(L + L_RED))[wave * 64 + 8 * ch + g] = red8_hi(acc, lane);
      BAR();
      unsigned c2[9];
#pragma unroll
      for (int i = 0; i < 9; ++i) c2[i] = ldh(p.c + 72 * wave + 8 * i + g);
#pragma unroll
      for (int i = 0; i < 9; ++i) { c2[i] = silu_bits(c2[i]); asm volatile("" : "+v"(c2[i])); }
      BAR();
      const u16* s2 = p.wm + (size_t)(72 * wave + g) * NMOD + 64 * (18 + u) + 8 * ch;
      v4u w2[9];
#pragma unroll
      for (int i = 0; i < 9; ++i) w2[i] = ldnt(s2 + (size_t)8 * i * NMOD);
      PIN();
#pragma unroll
      for (int k = 0; k < 8; ++k) acc[k] = 0.f;
#pragma unroll
      for (int i = 0; i < 9; ++i) row_fma(acc, w2[i], c2[i]);
      ((LAS float*)(L + L_RED))[wave * 64 + 8 * ch + g] = red8_hi(acc, lane);
      STAMP(3);
      BAR();
    } else {
      tag = make_tag(ld8(rs, WS_EP));
      BAR();
      if (lane < 32) {
        f2 s = {0.f, 0.f};
#pragma unroll
        for (int w = 0; w < 8; ++w) s += *(LAS f2*)(L + L_RED + w * 256 + lane * 8);
        const int cb = u % 18;
        st16(rs, WS_HG + (((cb / 9) * 2 + u / 18) * 288 + (cb % 9) * 32 + lane) * 16, v4u{__float_as_uint(s.x), tag.x, __float_as_uint(s.y), tag.y});
      }
      STAMP(2);
      poll_tag(rs, WS_QG + ((u * 7) & 63) * 256, tag);
      BAR();
      BAR();
      if (lane < 16) {
        f4 s = {0.f, 0.f, 0.f, 0.f};
#pragma unroll
        for (int w = 0; w < 8; ++w) s += *(LAS f4*)(L + L_RED + w * 256 + lane * 16);
        st16(rs, WS_MG + (u * 16 + lane) * 16, v4u{pk2(s.x, s.y), tag.x, pk2(s.z, s.w), tag.y});
      }
      STAMP(4);
    }
    FLUSH();
    return;
  } else if (j < 13) {
    const int qb = 8 * xc + (j - 5), kvh = qb >> 2, part = qb & 3, t = threadIdx.x;
    const int g = lane >> 3, ch = lane & 7;
    fi = 128 + qb;
    v2u ep = ld8(rs, WS_EP);
    unsigned xb = ldh(p.x + t);
    v4u wv[9];
    float ln;
    if (!helper) {
      const u16* src = p.wq + (size_t)(72 * wave + g) * NQ + 64 * qb + 8 * ch;
#pragma unroll
      for (int i = 0; i < 9; ++i) wv[i] = ldnt(src + (size_t)8 * i * NQ);
      PIN();
      asm volatile("" : "+v"(ep), "+v"(xb));
      tag = make_tag(ep);
      ln = ln_input(L, bf2f(xb), wave, lane);
      pair_rows(wv[0], wv[1]); pair_rows(wv[2], wv[3]); pair_rows(wv[4], wv[5]); pair_rows(wv[6], wv[7]); asm volatile("" : "+v"(wv[8]));
    } else {
      const v4u sv = {SENT, SENT, SENT, SENT};
      const int id = part * 36 + lane;
      st16(rs, WS_PO + kvh * 8192 + part * 2048 + lane * 16, sv);
      st16(rs, WS_PO + kvh * 8192 + part * 2048 + 1024 + lane * 16, sv);
      st16(rs, lane < 36 ? WS_AP + ((id >> 4) * 16 + kvh) * 256 + (id & 15) * 16 : lane < 38 ? WS_ML + kvh * 128 + part * 32 + (lane - 36) * 16 : 0x7FFFFFF0, sv);
      PIN();
      asm volatile("" : "+v"(ep), "+v"(xb));
      tag = make_tag(ep);
      ln = ln_input(L, bf2f(xb), wave, lane);
    }
    STAMP(1);
    {
      const int off = WS_HG + (t >> 1) * 16;
      v4u g0, g1, g2, g3;
      for (int s = 0; s < SPIN_CAP; ++s) {
        asm volatile("" ::: "memory");
        g0 = ld16(rs, off); g1 = ld16(rs, off + 288 * 16); g2 = ld16(rs, off + 2 * 288 * 16); g3 = ld16(rs, off + 3 * 288 * 16);
        if (__all(g0.y == tag.x && g0.w == tag.y && g1.y == tag.x && g1.w == tag.y && g2.y == tag.x && g2.w == tag.y && g3.y == tag.x && g3.w == tag.y)) break;
      }
      const bool odd = t & 1;
      const float sa = rb(__uint_as_float(odd ? g0.z : g0.x) + __uint_as_float(odd ? g1.z : g1.x));
      const float ca = rb(__uint_as_float(odd ? g2.z : g2.x) + __uint_as_float(odd ? g3.z : g3.x));
      const float h = rb(rb(ln * rb(1.f + ca)) + sa);
      const int hw = t / 72, rem = t - 72 * hw;
      *(LAS u16*)(L + L_HT + (hw * 8 + (rem & 7)) * 32 + (rem >> 3) * 2) = (u16)(__float_as_uint(h) >> 16);
    }
    STAMP(2);
    BAR();
    if (!helper) {
      const v4u hp = *(LAS v4u*)(L + L_HT + (wave * 8 + g) * 32);
      const unsigned h8 = *(LAS u16*)(L + L_HT + (wave * 8 + g) * 32 + 16);
      float acc[8] = {0, 0, 0, 0, 0, 0, 0, 0};
      pair_fma(acc, wv[0], wv[1], hp.x); pair_fma(acc, wv[2], wv[3], hp.y); pair_fma(acc, wv[4], wv[5], hp.z); pair_fma(acc, wv[6], wv[7], hp.w);
      row_fma(acc, wv[8], h8);
      ((LAS float*)(L + L_RED))[wave * 64 + 8 * ch + g] = red8_hi(acc, lane);
    }
    BAR();
    if (helper) {
      f4 s = {0.f, 0.f, 0.f, 0.f};
#pragma unroll
      for (int w = 0; w < 8; ++w) s += *(LAS f4*)(L + L_RED + w * 256 + (lane & 15) * 16);
      asm volatile("s_waitcnt vmcnt(0)" ::: "memory");
      if (lane < 16) st16(rs, WS_QG + (qb * 16 + lane) * 16, v4u{pk2(s.x, s.y), tag.x, pk2(s.z, s.w), tag.y});
      STAMP(3);
      poll_tag(rs, WS_QG + lane * 256, tag);
      STAMP(4);
    }
  } else if (j < 29) {
    const int kvh = 2 * xc + ((j - 13) >> 3), sp = (j - 13) & 7;
    fi = kvh * 8 + sp;
    if (!helper) {
      const int g = lane >> 4, c = lane & 15;
      const size_t kb = ((size_t)kvh * SK + (SK - W) + 256 * sp + 32 * wave) * DH;
      LAS unsigned char* kt = L + L_K + wave * 8192;
      {
        const unsigned vo = g * 256 + ((c ^ g) << 4), l0 = (unsigned)(uintptr_t)kt;
        const u16* kp = p.kc + kb;
        unsigned keep;
        asm volatile("s_mov_b32 %0, m0\n\ts_mov_b32 m0, %5\n\ts_nop 4\n\t"
                     "global_load_lds_dwordx4 %1, %7 nt\n\tglobal_load_lds_dwordx4 %2, %7 offset:1024 nt\n\t"
                     "global_load_lds_dwordx4 %3, %7 offset:2048 nt\n\tglobal_load_lds_dwordx4 %4, %7 offset:3072 nt\n\t"
                     "s_mov_b32 m0, %6\n\ts_nop 0\n\t"
                     "global_load_lds_dwordx4 %1, %8 nt\n\tglobal_load_lds_dwordx4 %2, %8 offset:1024 nt\n\t"
                     "global_load_lds_dwordx4 %3, %8 offset:2048 nt\n\tglobal_load_lds_dwordx4 %4, %8 offset:3072 nt\n\t"
                     "s_mov_b32 m0, %0"
                     : "=&s"(keep) : "v"(vo), "v"(vo ^ 64u), "v"(vo ^ 128u), "v"(vo ^ 192u), "s"(l0), "s"(l0 + 4096u), "s"(kp), "s"(kp + 2048) : "memory");
      }
      v4u vv[8], wv[4];
#pragma unroll
      for (int i = 0; i < 8; ++i) vv[i] = ldnt(p.vc + kb + (16 * (i >> 2) + 4 * g + (i & 3)) * DH + 8 * c);
      load_wo(p, wv, kvh, sp, wave, lane);
      PIN();
      asm volatile("s_waitcnt vmcnt(12)" ::: "memory");
      v4u kf[8];
#pragma unroll
      for (int T = 0; T < 2; ++T)
#pragma unroll
        for (int s = 0; s < 4; ++s) kf[T * 4 + s] = *(LAS v4u*)(kt + (16 * T + c) * 256 + ((4 * s + g) ^ c) * 16);
      STAMP(1);
      BAR();
      { const v2u tg = *(LAS v2u*)(L + L_TAG); tag.x = __builtin_amdgcn_readfirstlane(tg.x); tag.y = __builtin_amdgcn_readfirstlane(tg.y); }
      f4 d0 = {0, 0, 0, 0}, d1 = {0, 0, 0, 0};
#pragma unroll
      for (int s = 0; s < 4; ++s) {
        const v4u qf = *(LAS v4u*)(L + L_QS + (c & 1) * 256 + (4 * s + g) * 16);
        d0 = __builtin_amdgcn_mfma_f32_16x16x32_bf16(__builtin_bit_cast(bf8, kf[s]), __builtin_bit_cast(bf8, qf), d0, 0, 0, 0);
        d1 = __builtin_amdgcn_mfma_f32_16x16x32_bf16(__builtin_bit_cast(bf8, kf[4 + s]), __builtin_bit_cast(bf8, qf), d1, 0, 0, 0);
      }
      const f4 ds = (c & 8) ? d1 : d0;
      const float e0 = (c & 4) ? ds.z : ds.x, e1 = (c & 4) ? ds.w : ds.y;
      const float sc = rb(rb((c & 2) ? e1 : e0) * 0.08838834764831845f);
      float m = fmaxf(sc, dpp<X2>(sc)); m = fmaxf(m, dpp<R4>(m)); m = fmaxf(m, dpp<R8>(m)); m = mx32(mx16(m));
      if (lane < 2) ((LAS float*)(L + L_WM))[lane * 8 + wave] = m;
      BAR();
      {
        const f4 a = *(LAS f4*)(L + L_WM + (c & 1) * 32), b = *(LAS f4*)(L + L_WM + (c & 1) * 32 + 16);
        m = fmaxf(fmaxf(fmaxf(a.x, a.y), fmaxf(a.z, a.w)), fmaxf(fmaxf(b.x, b.y), fmaxf(b.z, b.w)));
      }
      const float pe = __builtin_amdgcn_exp2f((sc - m) * 1.4426950408889634f);
      v4u pa;
      pa.x = pk2(pe, dpp<0x102>(pe)); pa.y = dppu<0x104>(pa.x); pa.z = dppu<0x108>(pa.x); pa.w = dppu<0x10C>(pa.x);
      float l = pe + dpp<X2>(pe); l += dpp<R4>(l); l += dpp<R8>(l); l = sw16(l, l); l = sw32(l, l);
      if (lane < 2) ((LAS float*)(L + L_WL))[lane * 8 + wave] = l;
      STAMP(2);
      pair_rows(vv[0], vv[1]); pair_rows(vv[2], vv[3]); pair_rows(vv[4], vv[5]); pair_rows(vv[6], vv[7]);
      f4 o[8];
#pragma unroll
      for (int e = 0; e < 8; ++e) {
        const v4u bfr = {vv[0 + (e & 1)][e >> 1], vv[2 + (e & 1)][e >> 1], vv[4 + (e & 1)][e >> 1], vv[6 + (e & 1)][e >> 1]};
        o[e] = __builtin_amdgcn_mfma_f32_16x16x32_bf16(__builtin_bit_cast(bf8, pa), __builtin_bit_cast(bf8, bfr), f4{0, 0, 0, 0}, 0, 0, 0);
      }
      if (lane < 16) {
        LAS f4* ow = (LAS f4*)(L + L_OW + wave * 1024 + lane * 32);
        ow[0] = f4{o[0].x, o[1].x, o[2].x, o[3].x}; ow[1] = f4{o[4].x, o[5].x, o[6].x, o[7].x};
        ow[32] = f4{o[0].y, o[1].y, o[2].y, o[3].y}; ow[33] = f4{o[4].y, o[5].y, o[6].y, o[7].y};
      }
      STAMP(3);
      BAR();
      o_stage(rs, L, wv, kvh, wave, lane);
      STAMP(4);
      BAR();
    } else {
      tag = make_tag(ld8(rs, WS_EP));
      const v4u q = poll_tag(rs, WS_QG + (kvh * 64 + lane) * 16, tag);
      *(LAS v2u*)(L + L_QS + lane * 8) = v2u{q.x, q.z};
      if (lane == 0) *(LAS v2u*)(L + L_TAG) = tag;
      STAMP(1);
      BAR();
      BAR();
      BAR();
      f4 s = {0.f, 0.f, 0.f, 0.f};
#pragma unroll
      for (int w = 0; w < 8; ++w) s += *(LAS f4*)(L + L_OW + w * 1024 + lane * 16);
      st16(rs, WS_PO + (kvh * 8 + sp) * 1024 + lane * 16, v4u{nosent(s.x), nosent(s.y), nosent(s.z), nosent(s.w)});
      if (lane < 2) {
        const f4 a = *(LAS f4*)(L + L_WM + lane * 32), b = *(LAS f4*)(L + L_WM + lane * 32 + 16);
        const f4 c = *(LAS f4*)(L + L_WL + lane * 32), d = *(LAS f4*)(L + L_WL + lane * 32 + 16);
        const float m = fmaxf(fmaxf(fmaxf(a.x, a.y), fmaxf(a.z, a.w)), fmaxf(fmaxf(b.x, b.y), fmaxf(b.z, b.w)));
        const float l = ((c.x + c.y) + (c.z + c.w)) + ((d.x + d.y) + (d.z + d.w));
        st8(rs, WS_ML + ((kvh * 2 + lane) * 8 + sp) * 8, v2u{nosent(m), nosent(l)});
      }
      STAMP(3);
      poll_tag(rs, WS_QG + lane * 256, tag);
      BAR();
      publish_ap(rs, L, kvh, sp, lane);
      STAMP(4);
    }
  } else if (j < 31) {
    const int kvh = 2 * xc + (j - 29);
    if (!helper) {
      v4u wv[4];
      load_wo(p, wv, kvh, 8, wave, lane);
      PIN();
      BAR();
      o_stage(rs, L, wv, kvh, wave, lane);
      BAR();
    } else {
      tag = make_tag(ld8(rs, WS_EP));
      poll_tag(rs, WS_QG + (kvh * 64 + lane) * 16, tag);
      BAR();
      BAR();
      publish_ap(rs, L, kvh, 8, lane);
      STAMP(4);
    }
    FLUSH();
    return;
  } else {
    return;
  }

  {
    const int col = 64 * wave + 4 * (lane & 15) + (lane >> 4), d0 = 3 * fi;
    const unsigned xb = ldh(p.x + col);
    v4u w1v, b1v, w2v; unsigned w1e, b1e, w2e;
    const int tw = threadIdx.x;
    if (!helper) {
      w1v = ldg(p.w1 + 8 * tw); b1v = ldg(p.b1 + 8 * tw); w2v = ldg(p.w2 + 8 * tw);
      w1e = ldh(p.w1 + 4096 + tw); b1e = ldh(p.b1 + 4096 + tw); w2e = ldh(p.w2 + 4096 + tw);
      PIN();
    }
    const unsigned b2b = ldh(p.b2);
    PIN();
    float ga, sb, av, gb;
    {
      const int off = WS_MG + (16 * wave + (lane & 15)) * 16;
      v4u g0, g1, g2, g3;
      for (int s = 0; s < SPIN_CAP; ++s) {
        asm volatile("" ::: "memory");
        g0 = ld16(rs, off); g1 = ld16(rs, off + 144 * 16); g2 = ld16(rs, off + 2 * 144 * 16); g3 = ld16(rs, off + 3 * 144 * 16);
        if (__all(g0.y == tag.x && g0.w == tag.y && g1.y == tag.x && g1.w == tag.y && g2.y == tag.x && g2.w == tag.y && g3.y == tag.x && g3.w == tag.y)) break;
      }
      const bool hi = lane & 32; const int sh = (lane & 16) ? 0 : 16;
      ga = __uint_as_float(((hi ? g0.z : g0.x) << sh) & 0xFFFF0000u);
      sb = __uint_as_float(((hi ? g1.z : g1.x) << sh) & 0xFFFF0000u);
      av = rb(1.f + __uint_as_float(((hi ? g2.z : g2.x) << sh) & 0xFFFF0000u));
      gb = __uint_as_float(((hi ? g3.z : g3.x) << sh) & 0xFFFF0000u);
    }
    STAMP(5);
    {
      const float v[8] = {av, av * av, av * sb, sb, sb * sb, 0.f, 0.f, 0.f};
      const float r = sum_lo3(red8_hi(v, lane));
      if ((lane & 7) == 0) ((LAS float*)(L + L_ST0))[wave * 8 + (lane >> 3)] = r;
      const unsigned k = (unsigned)(col - d0);
      if (k < 3u) { LAS float* own = (LAS float*)(L + L_OWN); own[4 + k] = av; own[8 + k] = sb; own[12 + k] = gb; }
    }
    float A0[9], A1[9], A2[9], A3[9], U0[9], U1[9];
    if (!helper) {
      asm volatile("" : "+v"(w1e), "+v"(b1e), "+v"(w2e));
      const float c1 = -2.3022081986f, c2 = -0.1029432397f;
#pragma unroll
      for (int i = 0; i < 9; ++i) {
        const unsigned a = i < 8 ? w1v[i >> 1] : w1e, b = i < 8 ? b1v[i >> 1] : b1e, c = i < 8 ? w2v[i >> 1] : w2e;
        const float wa = (i & 1) ? __uint_as_float(a & 0xFFFF0000u) : bf2f(a), ba = (i & 1) ? __uint_as_float(b & 0xFFFF0000u) : bf2f(b), wb = (i & 1) ? __uint_as_float(c & 0xFFFF0000u) : bf2f(c);
        const float cw = c2 * wa * wa, cbb = c2 * ba * ba;
        A3[i] = cw * wa; A2[i] = 3.f * cw * ba; A1[i] = wa * (c1 + 3.f * cbb); A0[i] = ba * (c1 + cbb);
        U1[i] = wa * wb; U0[i] = ba * wb;
      }
    }
    BAR();
    float sA, sAA, sAS, sS, sSS;
    {
      float r = ((LAS float*)(L + L_ST0))[(lane & 7) * 8 + (lane >> 3)];
      r = sum_lo3(r) + ((LAS float*)(L + L_ST0))[64 + (lane >> 3)];
      sA = __uint_as_float(__builtin_amdgcn_readlane(__float_as_uint(r), 0)); sAA = __uint_as_float(__builtin_amdgcn_readlane(__float_as_uint(r), 8));
      sAS = __uint_as_float(__builtin_amdgcn_readlane(__float_as_uint(r), 16)); sS = __uint_as_float(__builtin_amdgcn_readlane(__float_as_uint(r), 24));
      sSS = __uint_as_float(__builtin_amdgcn_readlane(__float_as_uint(r), 32));
    }
    const float av_d = ((LAS float*)(L + L_OWN))[4 + (lane & 3)], sb_d = ((LAS float*)(L + L_OWN))[8 + (lane & 3)];
    STAMP(6);
    float x1;
    {
      const int off = WS_AP + (wave * 16 + (lane >> 4)) * 256 + (lane & 15) * 16;
      v4u a0, a1, a2, a3;
      for (int s = 0; s < SPIN_CAP; ++s) {
        asm volatile("" ::: "memory");
        a0 = ld16(rs, off); a1 = ld16(rs, off + 1024); a2 = ld16(rs, off + 2048); a3 = ld16(rs, off + 3072);
        const unsigned worst = max(max(max(max(a0.x, a0.y), max(a0.z, a0.w)), max(max(a1.x, a1.y), max(a1.z, a1.w))), max(max(max(a2.x, a2.y), max(a2.z, a2.w)), max(max(a3.x, a3.y), max(a3.z, a3.w))));
        if (__all(worst != SENT)) break;
      }
      STAMP(7);
      const f4 s = (__builtin_bit_cast(f4, a0) + __builtin_bit_cast(f4, a1)) + (__builtin_bit_cast(f4, a2) + __builtin_bit_cast(f4, a3));
      const float at = sw16(sw32(s.x, s.z), sw32(s.y, s.w));
      x1 = rb(bf2f(xb) + rb(ga * rb(at)));
      const float uu = x1 * av;
      const float v[8] = {x1, x1 * x1, uu, uu * uu, uu * av, uu * sb, 0.f, 0.f};
      const float r = sum_lo3(red8_hi(v, lane));
      if ((lane & 7) == 0) ((LAS float*)(L + L_ST1))[wave * 8 + (lane >> 3)] = r;
      const unsigned k = (unsigned)(col - d0);
      if (k < 3u) ((LAS float*)(L + L_OWN))[k] = x1;
    }
    BAR();
    float t;
    const float x1_d = ((LAS float*)(L + L_OWN))[lane & 3];
    {
      float r = ((LAS float*)(L + L_ST1))[(lane & 7) * 8 + (lane >> 3)];
      r = sum_lo3(r) + ((LAS float*)(L + L_ST1))[64 + (lane >> 3)];
      const float sX = __uint_as_float(__builtin_amdgcn_readlane(__float_as_uint(r), 0)), sXX = __uint_as_float(__builtin_amdgcn_readlane(__float_as_uint(r), 8));
      const float sU = __uint_as_float(__builtin_amdgcn_readlane(__float_as_uint(r), 16)), sUU = __uint_as_float(__builtin_amdgcn_readlane(__float_as_uint(r), 24));
      const float sUA = __uint_as_float(__builtin_amdgcn_readlane(__float_as_uint(r), 32)), sUS = __uint_as_float(__builtin_amdgcn_readlane(__float_as_uint(r), 40));
      const float mean = sX * (1.f / D), var = fmaxf(sXX * (1.f / D) - mean * mean, 0.f);
      const float m1 = rb(mean), sq1 = rb(__builtin_sqrtf(rb(rb(var) + 1e-5f))), r1 = 1.f / sq1;
      const float mu = (r1 * (sU - m1 * sA) + sS) * (1.f / D);
      const float e2 = (r1 * r1 * (sUU - 2.f * m1 * sUA + m1 * m1 * sAA) + 2.f * r1 * (sUS - m1 * sAS) + sSS) * (1.f / D);
      const float var2 = fmaxf(e2 - mu * mu, 0.f);
      const float m2 = rb(mu), sq2 = rb(__builtin_sqrtf(rb(rb(var2) + 1e-5f)));
      const float z = rb(rb(x1_d - m1) / sq1);
      const float h = rb(rb(z * av_d) + sb_d);
      t = rb(rb(h - m2) / sq2);
    }
    STAMP(8);
    if (!helper) {
      float acc[4] = {0.f, 0.f, 0.f, 0.f};
#pragma unroll
      for (int d = 0; d < 3; ++d) {
        const float td = __uint_as_float(__builtin_amdgcn_readlane(__float_as_uint(t), d));
#pragma unroll
        for (int i = 0; i < 9; ++i) {
          const float a = fmaf(fmaf(fmaf(A3[i], td, A2[i]), td, A1[i]), td, A0[i]);
          const float r = __builtin_amdgcn_rcpf(1.f + __builtin_amdgcn_exp2f(a));
          acc[d] = fmaf(fmaf(U1[i], td, U0[i]), r, acc[d]);
        }
      }
      float r = sw16(sw32(acc[0], acc[2]), sw32(acc[1], acc[3]));
      r += dpp<R8>(r); r = sum_lo3(r);
      if ((lane & 15) == 0) ((LAS float*)(L + L_FIN))[wave * 4 + (lane >> 4)] = r;
    }
    STAMP(9);
    BAR();
    if (helper) {
      if (lane < 3) {
        float y = 0.f;
#pragma unroll
        for (int w = 0; w < 8; ++w) y += ((LAS float*)(L + L_FIN))[w * 4 + lane];
        y = rb(rb(y) + bf2f(b2b));
        const float gbd = ((LAS float*)(L + L_OWN))[12 + lane];
        *(GAS u16*)(p.out + d0 + lane) = (u16)(__float_as_uint(rb(x1_d + rb(gbd * y))) >> 16);
      }
      if (fi == 0 && lane == 0) st8(rs, WS_EP, tag);
      STAMP(10);
    }
    FLUSH();
  }
}

extern "C" void kernel_launch(void* const* d_in, const int* in_sizes, int n_in, void* d_out, int out_size, void* d_ws, size_t ws_size, hipStream_t stream) {
  Params p;
  p.x = (const u16*)d_in[0]; p.kc = (const u16*)d_in[1]; p.vc = (const u16*)d_in[2]; p.wq = (const u16*)d_in[3]; p.wo = (const u16*)d_in[4];
  p.w1 = (const u16*)d_in[6]; p.b1 = (const u16*)d_in[7]; p.w2 = (const u16*)d_in[8]; p.b2 = (const u16*)d_in[9];
  p.c = (const u16*)d_in[10]; p.wm = (const u16*)d_in[11];
  p.out = (u16*)d_out; p.ws = (unsigned char*)d_ws;
  fused_k<<<NWG, NTHR, 0, stream>>>(p);
}
```

```cpp
#include <hip/hip_runtime.h>
#include <stdint.h>

typedef unsigned short u16;
typedef unsigned long long u64;
typedef unsigned v4u __attribute__((ext_vector_type(4)));
typedef unsigned v2u __attribute__((ext_vector_type(2)));
typedef float f4 __attribute__((ext_vector_type(4)));
typedef float f2 __attribute__((ext_vector_type(2)));
typedef __bf16 bf2 __attribute__((ext_vector_type(2)));
typedef __bf16 bf8 __attribute__((ext_vector_type(8)));
#define GAS __attribute__((address_space(1)))
#define LAS __attribute__((address_space(3)))

constexpr int D = 576, NMOD = 6 * D, SK = 8192, DH = 128, W = 2048, F = 4608, NQ = 4096;
constexpr int NWG = 256, NTHR = 576;
constexpr int WS_EP = 0;
constexpr int WS_HG = 256;
constexpr int WS_QG = WS_HG + 4 * 288 * 16;
constexpr int WS_MG = WS_QG + 1024 * 16;
constexpr int WS_AP = WS_MG + 576 * 16;
constexpr int PO_REC = 1152;
constexpr int WS_PO = WS_AP + 9 * 16 * 256;
constexpr int WS_END = WS_PO + 16 * 8 * PO_REC;
constexpr int CODE_BYTES = 17152;
constexpr unsigned SENT = 0xFFFFFFFFu;
constexpr int SPIN_CAP = 1 << 17;
constexpr int AUX_SC1 = 16;

#define STAMP(i) do { } while (0)
#define STAMP_DECL
#define FLUSH() do { } while (0)

struct Params {
  const u16 *x, *kc, *vc, *wq, *wo, *w1, *b1, *w2, *b2, *c, *wm;
  u16* out;
  unsigned char* ws;
};

constexpr int L_K = 0;
constexpr int L_RED = 65536;
constexpr int L_HT = L_RED + 2048;
constexpr int L_QS = L_HT + 2048;
constexpr int L_WM = L_QS + 512;
constexpr int L_WL = L_WM + 64;
constexpr int L_OW = L_WL + 64;
constexpr int L_ST0 = L_OW + 8192;
constexpr int L_ST1 = L_ST0 + 320;
constexpr int L_OWN = L_ST1 + 320;
constexpr int L_FIN = L_OWN + 64;
constexpr int L_TAG = L_FIN + 128;
constexpr int L_END = L_TAG + 16;
constexpr int L_BYTES = 96 * 1024;
static_assert(L_END <= L_BYTES, "lds");

__device__ __forceinline__ float bf2f(unsigned h) { return __uint_as_float(h << 16); }
__device__ __forceinline__ float rb(float x) { f2 v = {0.f, x}; return __builtin_bit_cast(float, __builtin_convertvector(v, bf2)); }
__device__ __forceinline__ unsigned pk2(float lo, float hi) { f2 v = {lo, hi}; return __builtin_bit_cast(unsigned, __builtin_convertvector(v, bf2)); }
__device__ __forceinline__ float dot2(unsigned a, unsigned b, float c) { return __builtin_amdgcn_fdot2_f32_bf16(__builtin_bit_cast(bf2, a), __builtin_bit_cast(bf2, b), c, false); }
template <int C> __device__ __forceinline__ unsigned dppu(unsigned x) { return (unsigned)__builtin_amdgcn_mov_dpp((int)x, C, 0xf, 0xf, true); }
template <int C> __device__ __forceinline__ float dpp(float x) { return __uint_as_float(dppu<C>(__float_as_uint(x))); }
constexpr int X1 = 0xB1, X2 = 0x4E, X7 = 0x141, R4 = 0x124, R8 = 0x128;
__device__ __forceinline__ float sw32(float a, float b) { auto r = __builtin_amdgcn_permlane32_swap(__float_as_uint(a), __float_as_uint(b), false, false); return __uint_as_float(r[0]) + __uint_as_float(r[1]); }
__device__ __forceinline__ float sw16(float a, float b) { auto r = __builtin_amdgcn_permlane16_swap(__float_as_uint(a), __float_as_uint(b), false, false); return __uint_as_float(r[0]) + __uint_as_float(r[1]); }
__device__ __forceinline__ float mx32(float a) { auto r = __builtin_amdgcn_permlane32_swap(__float_as_uint(a), __float_as_uint(a), false, false); return fmaxf(__uint_as_float(r[0]), __uint_as_float(r[1])); }
__device__ __forceinline__ float mx16(float a) { auto r = __builtin_amdgcn_permlane16_swap(__float_as_uint(a), __float_as_uint(a), false, false); return fmaxf(__uint_as_float(r[0]), __uint_as_float(r[1])); }
__device__ __forceinline__ float red8_hi(const float (&a)[8], int lane) {
  float v0 = sw32(a[0], a[4]), v1 = sw32(a[1], a[5]), v2 = sw32(a[2], a[6]), v3 = sw32(a[3], a[7]);
  float u0 = sw16(v0, v2), u1 = sw16(v1, v3);
  float t0 = u0 + dpp<R8>(u0), t1 = u1 + dpp<R8>(u1);
  return (lane & 8) ? t1 : t0;
}
__device__ __forceinline__ float sum_lo3(float v) { v += dpp<X1>(v); v += dpp<X2>(v); v += dpp<X7>(v); return v; }
__device__ __forceinline__ float max_lo3(float v) { v = fmaxf(v, dpp<X1>(v)); v = fmaxf(v, dpp<X2>(v)); v = fmaxf(v, dpp<X7>(v)); return v; }
__device__ __forceinline__ unsigned nosent(float v) { unsigned u = __float_as_uint(v); return u == SENT ? 0x7FC00000u : u; }
__device__ __forceinline__ v4u ldnt(const void* p) { return __builtin_nontemporal_load((const GAS v4u*)p); }
__device__ __forceinline__ v4u ldg(const void* p) { return *(const GAS v4u*)p; }
__device__ __forceinline__ unsigned ldh(const u16* p) { return *(const GAS u16*)p; }
#define BAR() do { asm volatile("s_waitcnt lgkmcnt(0)" ::: "memory"); __builtin_amdgcn_s_barrier(); asm volatile("" ::: "memory"); } while (0)
#define PIN() __builtin_amdgcn_sched_barrier(0)
typedef __amdgpu_buffer_rsrc_t rsrc_t;
__device__ __forceinline__ v4u ld16(rsrc_t r, int off) { return __builtin_amdgcn_raw_buffer_load_b128(r, off, 0, AUX_SC1); }
__device__ __forceinline__ v2u ld8(rsrc_t r, int off) { return __builtin_amdgcn_raw_buffer_load_b64(r, off, 0, AUX_SC1); }
__device__ __forceinline__ void st16(rsrc_t r, int off, v4u v) { __builtin_amdgcn_raw_buffer_store_b128(v, r, off, 0, AUX_SC1); }
__device__ __forceinline__ void st8(rsrc_t r, int off, v2u v) { __builtin_amdgcn_raw_buffer_store_b64(v, r, off, 0, AUX_SC1); }
__device__ __forceinline__ void st4(rsrc_t r, int off, unsigned v) { __builtin_amdgcn_raw_buffer_store_b32(v, r, off, 0, AUX_SC1); }
__device__ __forceinline__ v2u make_tag(v2u e) {
  return v2u{__builtin_amdgcn_readfirstlane(e.x) + 0x9E3779B9u, __builtin_amdgcn_readfirstlane(e.y) + 0x7F4A7C15u};
}
__device__ __forceinline__ v4u poll_tag(rsrc_t r, int off, v2u tag) {
  v4u g;
  for (int s = 0; s < SPIN_CAP; ++s) {
    asm volatile("" ::: "memory");
    g = ld16(r, off);
    if (__all(g.y == tag.x && g.w == tag.y)) break;
  }
  return g;
}

__device__ __forceinline__ float rdl(float v, int l) { return __uint_as_float(__builtin_amdgcn_readlane(__float_as_uint(v), l)); }
__device__ __forceinline__ bool has_sent(v4u a) { return max(max(a.x, a.y), max(a.z, a.w)) == SENT; }
__device__ __forceinline__ bool tag_bad(v4u g, v2u tag) { return (g.y != tag.x) | (g.w != tag.y); }
__device__ __forceinline__ f2 gelu2(f2 A3, f2 A2, f2 A1, f2 A0, f2 U1, f2 U0, f2 t, f2 acc) {
  const f2 a = __builtin_elementwise_fma(__builtin_elementwise_fma(__builtin_elementwise_fma(A3, t, A2), t, A1), t, A0);
  const f2 d = f2{__builtin_amdgcn_exp2f(a.x), __builtin_amdgcn_exp2f(a.y)} + 1.f;
  return __builtin_elementwise_fma(__builtin_elementwise_fma(U1, t, U0), f2{__builtin_amdgcn_rcpf(d.x), __builtin_amdgcn_rcpf(d.y)}, acc);
}
__device__ __forceinline__ void poll_tag4(rsrc_t r, int off, int stride, v2u tag, v4u& g0, v4u& g1, v4u& g2, v4u& g3) {
  g0 = ld16(r, off); g1 = ld16(r, off + stride); g2 = ld16(r, off + 2 * stride); g3 = ld16(r, off + 3 * stride);
  for (int s = 0; s < SPIN_CAP; ++s) {
    const bool k0 = __any(tag_bad(g0, tag)), k1 = __any(tag_bad(g1, tag)), k2 = __any(tag_bad(g2, tag)), k3 = __any(tag_bad(g3, tag));
    if (!(k0 | k1 | k2 | k3)) break;
    asm volatile("" ::: "memory");
    if (k0) g0 = ld16(r, off);
    if (k1) g1 = ld16(r, off + stride);
    if (k2) g2 = ld16(r, off + 2 * stride);
    if (k3) g3 = ld16(r, off + 3 * stride);
  }
}

__device__ __forceinline__ void row_fma(float (&acc)[8], v4u w, unsigned slo) {
  const unsigned shi = slo << 16;
#pragma unroll
  for (int k = 0; k < 4; ++k) { acc[2 * k] = dot2(w[k], slo, acc[2 * k]); acc[2 * k + 1] = dot2(w[k], shi, acc[2 * k + 1]); }
}
__device__ __forceinline__ void pair_rows(v4u& a, v4u& b) {
#pragma unroll
  for (int k = 0; k < 4; ++k) { const unsigned lo = __builtin_amdgcn_perm(b[k], a[k], 0x05040100u), hi = __builtin_amdgcn_perm(b[k], a[k], 0x07060302u); a[k] = lo; b[k] = hi; }
}
__device__ __forceinline__ void pair_fma(float (&acc)[8], v4u lo, v4u hi, unsigned s2) {
#pragma unroll
  for (int k = 0; k < 4; ++k) { acc[2 * k] = dot2(lo[k], s2, acc[2 * k]); acc[2 * k + 1] = dot2(hi[k], s2, acc[2 * k + 1]); }
}
__device__ __forceinline__ unsigned silu_bits(unsigned cb) {
  const float c = bf2f(cb);
  return __float_as_uint(rb(c * __builtin_amdgcn_rcpf(1.f + __builtin_amdgcn_exp2f(c * -1.4426950408889634f)))) >> 16;
}

__device__ __forceinline__ float ln_input(LAS unsigned char* L, float xv, int wave, int lane) {
  float v = sw32(xv, xv * xv); v += dpp<R8>(v); v = sw16(v, v); v = sum_lo3(v);
  if ((lane & 31) == 0) ((LAS float*)(L + L_ST0))[wave * 8 + (lane >> 5)] = v;
  BAR();
  float s1 = 0.f, s2 = 0.f;
#pragma unroll
  for (int w = 0; w < 9; ++w) { const f2 u = *(LAS f2*)(L + L_ST0 + w * 32); s1 += u.x; s2 += u.y; }
  const float mean = s1 * (1.f / D), var = fmaxf(s2 * (1.f / D) - mean * mean, 0.f);
  const float sq = rb(__builtin_amdgcn_sqrtf(rb(rb(var) + 1e-5f)));
  return rb(rb(xv - rb(mean)) * __builtin_amdgcn_rcpf(sq));
}

__device__ __forceinline__ void o_stage(rsrc_t rs, LAS unsigned char* L, v4u (&wv)[4], int kvh, int wave, int lane) {
  pair_rows(wv[0], wv[1]); pair_rows(wv[2], wv[3]);
  const int hd = wave >> 2, sp = lane & 7;
  const int offo = WS_PO + (kvh * 8 + sp) * PO_REC + hd * 512 + (wave & 3) * 128 + (lane >> 3) * 16;
  const int offm = WS_PO + (kvh * 8 + sp) * PO_REC + 1024 + hd * 8;
  v4u po; v2u ml;
  for (int s = 0; s < SPIN_CAP; ++s) {
    asm volatile("" ::: "memory");
    po = ld16(rs, offo); ml = ld8(rs, offm);
    if (__all(max(max(max(po.x, po.y), max(po.z, po.w)), max(ml.x, ml.y)) != SENT)) break;
  }
  const float m = __uint_as_float(ml.x), M = max_lo3(m);
  const float e = __builtin_amdgcn_exp2f((m - M) * 1.4426950408889634f);
  const float Ls = sum_lo3(e * __uint_as_float(ml.y));
  const float wgt = e * __builtin_amdgcn_rcpf(Ls);
  float o[4];
#pragma unroll
  for (int k = 0; k < 4; ++k) o[k] = sum_lo3(wgt * __uint_as_float(po[k]));
  float acc[8] = {0, 0, 0, 0, 0, 0, 0, 0};
  pair_fma(acc, wv[0], wv[1], pk2(o[0], o[1])); pair_fma(acc, wv[2], wv[3], pk2(o[2], o[3]));
  const float r = red8_hi(acc, lane);
  ((LAS float*)(L + L_RED))[wave * 64 + 8 * (lane & 7) + (lane >> 3)] = r;
}
__device__ __forceinline__ void load_wo(const Params& p, v4u (&wv)[4], int kvh, int cb, int wave, int lane) {
  const u16* src = p.wo + (size_t)((2 * kvh + (wave >> 2)) * DH + 32 * (wave & 3) + 4 * (lane >> 3)) * D + 64 * cb + 8 * (lane & 7);
#pragma unroll
  for (int i = 0; i < 4; ++i) wv[i] = ldnt(src + i * D);
}
__device__ __forceinline__ void publish_ap(rsrc_t rs, LAS unsigned char* L, int kvh, int cb, int lane) {
  float s = 0.f;
#pragma unroll
  for (int w = 0; w < 8; ++w) s += ((LAS float*)(L + L_RED))[w * 64 + lane];
  st4(rs, WS_AP + (cb * 16 + kvh) * 256 + lane * 4, nosent(s));
}

__global__ void __launch_bounds__(NTHR) fused_k(Params p) {
  __shared__ __attribute__((aligned(16))) unsigned char smem[L_BYTES];
  LAS unsigned char* L = (LAS unsigned char*)smem;
  u64 pc0;
  asm volatile("s_getpc_b64 %0" : "=s"(pc0));
  const int lane = threadIdx.x & 63, wave = __builtin_amdgcn_readfirstlane(threadIdx.x >> 6);
  const int xc = blockIdx.x & 7, j = blockIdx.x >> 3;
  const bool helper = wave == 8;
  const rsrc_t rs = __builtin_amdgcn_make_buffer_rsrc((void*)p.ws, 0, WS_END, 0x00020000);
  STAMP_DECL;
  STAMP(0);
  int fi;
  v2u tag;
  if (j == 31) {
    const unsigned char* pc = (const unsigned char*)(pc0 & ~127ull);
    const int o = threadIdx.x * 16;
    v4u a = ldg(pc + o), b = ldg(pc + (o + NTHR * 16 < CODE_BYTES ? o + NTHR * 16 : o));
    asm volatile("" :: "v"(a), "v"(b));
    return;
  }
  if (j < 5) {
    const int u = xc + 8 * j;
    if (u >= 36) return;
    const int g = lane >> 3, ch = lane & 7;
    if (!helper) {
      const int cb = u % 18, r0 = 288 * (u / 18) + 36 * wave + g;
      unsigned cv[5]; v4u wv[5];
#pragma unroll
      for (int i = 0; i < 4; ++i) cv[i] = ldh(p.c + r0 + 8 * i);
      cv[4] = ldh(p.c + r0 + (g < 4 ? 32 : 0));
      const u16* src = p.wm + (size_t)r0 * NMOD + 64 * cb + 8 * ch;
#pragma unroll
      for (int i = 0; i < 4; ++i) wv[i] = ldnt(src + (size_t)8 * i * NMOD);
      { const rsrc_t rw = __builtin_amdgcn_make_buffer_rsrc((void*)p.wm, 0, D * NMOD * 2, 0x00020000);
        wv[4] = __builtin_amdgcn_raw_buffer_load_b128(rw, g < 4 ? ((r0 + 32) * NMOD + 64 * cb + 8 * ch) * 2 : 0x7FFFFFF0, 0, 2); }
      PIN();
      float acc[8] = {0, 0, 0, 0, 0, 0, 0, 0};
#pragma unroll
      for (int i = 0; i < 5; ++i) cv[i] = silu_bits(cv[i]);
#pragma unroll
      for (int i = 0; i < 5; ++i) row_fma(acc, wv[i], cv[i]);
      STAMP(1);
      ((LAS float*)(L + L_RED))[wave * 64 + 8 * ch + g] = red8_hi(acc, lane);
      BAR();
      unsigned c2[9];
#pragma unroll
      for (int i = 0; i < 9; ++i) c2[i] = ldh(p.c + 72 * wave + 8 * i + g);
#pragma unroll
      for (int i = 0; i < 9; ++i) { c2[i] = silu_bits(c2[i]); asm volatile("" : "+v"(c2[i])); }
      BAR();
      const u16* s2 = p.wm + (size_t)(72 * wave + g) * NMOD + 64 * (18 + u) + 8 * ch;
      v4u w2[9];
#pragma unroll
      for (int i = 0; i < 9; ++i) w2[i] = ldnt(s2 + (size_t)8 * i * NMOD);
      PIN();
#pragma unroll
      for (int k = 0; k < 8; ++k) acc[k] = 0.f;
#pragma unroll
      for (int i = 0; i < 9; ++i) row_fma(acc, w2[i], c2[i]);
      ((LAS float*)(L + L_RED))[wave * 64 + 8 * ch + g] = red8_hi(acc, lane);
      STAMP(3);
      BAR();
    } else {
      tag = make_tag(ld8(rs, WS_EP));
      BAR();
      if (lane < 32) {
        f2 s = {0.f, 0.f};
#pragma unroll
        for (int w = 0; w < 8; ++w) s += *(LAS f2*)(L + L_RED + w * 256 + lane * 8);
        const int cb = u % 18;
        st16(rs, WS_HG + (((cb / 9) * 2 + u / 18) * 288 + (cb % 9) * 32 + lane) * 16, v4u{__float_as_uint(s.x), tag.x, __float_as_uint(s.y), tag.y});
      }
      STAMP(2);
      poll_tag(rs, WS_QG + ((u * 7) & 63) * 256, tag);
      BAR();
      BAR();
      if (lane < 16) {
        f4 s = {0.f, 0.f, 0.f, 0.f};
#pragma unroll
        for (int w = 0; w < 8; ++w) s += *(LAS f4*)(L + L_RED + w * 256 + lane * 16);
        st16(rs, WS_MG + (u * 16 + lane) * 16, v4u{pk2(s.x, s.y), tag.x, pk2(s.z, s.w), tag.y});
      }
      STAMP(4);
    }
    FLUSH();
    return;
  } else if (j < 13) {
    const int qb = 8 * xc + (j - 5), kvh = qb >> 2, part = qb & 3, t = threadIdx.x;
    const int g = lane >> 3, ch = lane & 7;
    fi = 128 + qb;
    v2u ep = ld8(rs, WS_EP);
    unsigned xb = ldh(p.x + t);
    v4u wv[9];
    float ln;
    if (!helper) {
      const u16* src = p.wq + (size_t)(72 * wave + g) * NQ + 64 * qb + 8 * ch;
#pragma unroll
      for (int i = 0; i < 9; ++i) wv[i] = ldnt(src + (size_t)8 * i * NQ);
      PIN();
      asm volatile("" : "+v"(ep), "+v"(xb));
      tag = make_tag(ep);
      ln = ln_input(L, bf2f(xb), wave, lane);
      pair_rows(wv[0], wv[1]); pair_rows(wv[2], wv[3]); pair_rows(wv[4], wv[5]); pair_rows(wv[6], wv[7]); asm volatile("" : "+v"(wv[8]));
    } else {
      const v4u sv = {SENT, SENT, SENT, SENT};
      const int id = part * 36 + lane;
      const int rec = WS_PO + (kvh * 8 + part * 2) * PO_REC;
      st16(rs, rec + lane * 16, sv);
      st16(rs, rec + PO_REC + lane * 16, sv);
      st16(rs, lane < 36 ? WS_AP + ((id >> 4) * 16 + kvh) * 256 + (id & 15) * 16 : lane < 38 ? rec + (lane - 36) * PO_REC + 1024 : 0x7FFFFFF0, sv);
      PIN();
      asm volatile("" : "+v"(ep), "+v"(xb));
      tag = make_tag(ep);
      ln = ln_input(L, bf2f(xb), wave, lane);
    }
    STAMP(1);
    {
      const int off = WS_HG + (t >> 1) * 16;
      v4u g0, g1, g2, g3;
      poll_tag4(rs, off, 288 * 16, tag, g0, g1, g2, g3);
      const bool odd = t & 1;
      const float sa = rb(__uint_as_float(odd ? g0.z : g0.x) + __uint_as_float(odd ? g1.z : g1.x));
      const float ca = rb(__uint_as_float(odd ? g2.z : g2.x) + __uint_as_float(odd ? g3.z : g3.x));
      const float h = rb(rb(ln * rb(1.f + ca)) + sa);
      const int hw = t / 72, rem = t - 72 * hw;
      *(LAS u16*)(L + L_HT + (hw * 8 + (rem & 7)) * 32 + (rem >> 3) * 2) = (u16)(__float_as_uint(h) >> 16);
    }
    STAMP(2);
    BAR();
    if (!helper) {
      const v4u hp = *(LAS v4u*)(L + L_HT + (wave * 8 + g) * 32);
      const unsigned h8 = *(LAS u16*)(L + L_HT + (wave * 8 + g) * 32 + 16);
      float acc[8] = {0, 0, 0, 0, 0, 0, 0, 0};
      pair_fma(acc, wv[0], wv[1], hp.x); pair_fma(acc, wv[2], wv[3], hp.y); pair_fma(acc, wv[4], wv[5], hp.z); pair_fma(acc, wv[6], wv[7], hp.w);
      row_fma(acc, wv[8], h8);
      ((LAS float*)(L + L_RED))[wave * 64 + 8 * ch + g] = red8_hi(acc, lane);
    }
    BAR();
    if (helper) {
      f4 s = {0.f, 0.f, 0.f, 0.f};
#pragma unroll
      for (int w = 0; w < 8; ++w) s += *(LAS f4*)(L + L_RED + w * 256 + (lane & 15) * 16);
      asm volatile("s_waitcnt vmcnt(0)" ::: "memory");
      if (lane < 16) st16(rs, WS_QG + (qb * 16 + lane) * 16, v4u{pk2(s.x, s.y), tag.x, pk2(s.z, s.w), tag.y});
      STAMP(3);
      poll_tag(rs, WS_QG + lane * 256, tag);
      STAMP(4);
    }
  } else if (j < 29) {
    const int kvh = 2 * xc + ((j - 13) >> 3), sp = (j - 13) & 7;
    fi = kvh * 8 + sp;
    if (!helper) {
      const int g = lane >> 4, c = lane & 15;
      const size_t kb = ((size_t)kvh * SK + (SK - W) + 256 * sp + 32 * wave) * DH;
      LAS unsigned char* kt = L + L_K + wave * 8192;
      {
        const unsigned vo = g * 256 + ((c ^ g) << 4), l0 = (unsigned)(uintptr_t)kt;
        const u16* kp = p.kc + kb;
        unsigned keep;
        asm volatile("s_mov_b32 %0, m0\n\ts_mov_b32 m0, %5\n\ts_nop 4\n\t"
                     "global_load_lds_dwordx4 %1, %7 nt\n\tglobal_load_lds_dwordx4 %2, %7 offset:1024 nt\n\t"
                     "global_load_lds_dwordx4 %3, %7 offset:2048 nt\n\tglobal_load_lds_dwordx4 %4, %7 offset:3072 nt\n\t"
                     "s_mov_b32 m0, %6\n\ts_nop 0\n\t"
                     "global_load_lds_dwordx4 %1, %8 nt\n\tglobal_load_lds_dwordx4 %2, %8 offset:1024 nt\n\t"
                     "global_load_lds_dwordx4 %3, %8 offset:2048 nt\n\tglobal_load_lds_dwordx4 %4, %8 offset:3072 nt\n\t"
                     "s_mov_b32 m0, %0"
                     : "=&s"(keep) : "v"(vo), "v"(vo ^ 64u), "v"(vo ^ 128u), "v"(vo ^ 192u), "s"(l0), "s"(l0 + 4096u), "s"(kp), "s"(kp + 2048) : "memory");
      }
      v4u vv[8], wv[4];
#pragma unroll
      for (int i = 0; i < 8; ++i) vv[i] = ldnt(p.vc + kb + (16 * (i >> 2) + 4 * g + (i & 3)) * DH + 8 * c);
      load_wo(p, wv, kvh, sp, wave, lane);
      PIN();
      asm volatile("s_waitcnt vmcnt(12)" ::: "memory");
      v4u kf[8];
#pragma unroll
      for (int T = 0; T < 2; ++T)
#pragma unroll
        for (int s = 0; s < 4; ++s) kf[T * 4 + s] = *(LAS v4u*)(kt + (16 * T + c) * 256 + ((4 * s + g) ^ c) * 16);
      STAMP(1);
      BAR();
      { const v2u tg = *(LAS v2u*)(L + L_TAG); tag.x = __builtin_amdgcn_readfirstlane(tg.x); tag.y = __builtin_amdgcn_readfirstlane(tg.y); }
      f4 d0 = {0, 0, 0, 0}, d1 = {0, 0, 0, 0};
#pragma unroll
      for (int s = 0; s < 4; ++s) {
        const v4u qf = *(LAS v4u*)(L + L_QS + (c & 1) * 256 + (4 * s + g) * 16);
        d0 = __builtin_amdgcn_mfma_f32_16x16x32_bf16(__builtin_bit_cast(bf8, kf[s]), __builtin_bit_cast(bf8, qf), d0, 0, 0, 0);
        d1 = __builtin_amdgcn_mfma_f32_16x16x32_bf16(__builtin_bit_cast(bf8, kf[4 + s]), __builtin_bit_cast(bf8, qf), d1, 0, 0, 0);
      }
      const f4 ds = (c & 8) ? d1 : d0;
      const float e0 = (c & 4) ? ds.z : ds.x, e1 = (c & 4) ? ds.w : ds.y;
      const float sc = rb(rb((c & 2) ? e1 : e0) * 0.08838834764831845f);
      float m = fmaxf(sc, dpp<X2>(sc)); m = fmaxf(m, dpp<R4>(m)); m = fmaxf(m, dpp<R8>(m)); m = mx32(mx16(m));
      if (lane < 2) ((LAS float*)(L + L_WM))[lane * 8 + wave] = m;
      BAR();
      {
        const f4 a = *(LAS f4*)(L + L_WM + (c & 1) * 32), b = *(LAS f4*)(L + L_WM + (c & 1) * 32 + 16);
        m = fmaxf(fmaxf(fmaxf(a.x, a.y), fmaxf(a.z, a.w)), fmaxf(fmaxf(b.x, b.y), fmaxf(b.z, b.w)));
      }
      const float pe = __builtin_amdgcn_exp2f((sc - m) * 1.4426950408889634f);
      v4u pa;
      pa.x = pk2(pe, dpp<0x102>(pe)); pa.y = dppu<0x104>(pa.x); pa.z = dppu<0x108>(pa.x); pa.w = dppu<0x10C>(pa.x);
      float l = pe + dpp<X2>(pe); l += dpp<R4>(l); l += dpp<R8>(l); l = sw16(l, l); l = sw32(l, l);
      if (lane < 2) ((LAS float*)(L + L_WL))[lane * 8 + wave] = l;
      STAMP(2);
      pair_rows(vv[0], vv[1]); pair_rows(vv[2], vv[3]); pair_rows(vv[4], vv[5]); pair_rows(vv[6], vv[7]);
      f4 o[8];
#pragma unroll
      for (int e = 0; e < 8; ++e) {
        const v4u bfr = {vv[0 + (e & 1)][e >> 1], vv[2 + (e & 1)][e >> 1], vv[4 + (e & 1)][e >> 1], vv[6 + (e & 1)][e >> 1]};
        o[e] = __builtin_amdgcn_mfma_f32_16x16x32_bf16(__builtin_bit_cast(bf8, pa), __builtin_bit_cast(bf8, bfr), f4{0, 0, 0, 0}, 0, 0, 0);
      }
      if (lane < 16) {
        LAS f4* ow = (LAS f4*)(L + L_OW + wave * 1024 + lane * 32);
        ow[0] = f4{o[0].x, o[1].x, o[2].x, o[3].x}; ow[1] = f4{o[4].x, o[5].x, o[6].x, o[7].x};
        ow[32] = f4{o[0].y, o[1].y, o[2].y, o[3].y}; ow[33] = f4{o[4].y, o[5].y, o[6].y, o[7].y};
      }
      STAMP(3);
      BAR();
      o_stage(rs, L, wv, kvh, wave, lane);
      STAMP(4);
      BAR();
    } else {
      tag = make_tag(ld8(rs, WS_EP));
      const v4u q = poll_tag(rs, WS_QG + (kvh * 64 + lane) * 16, tag);
      *(LAS v2u*)(L + L_QS + lane * 8) = v2u{q.x, q.z};
      if (lane == 0) *(LAS v2u*)(L + L_TAG) = tag;
      STAMP(1);
      BAR();
      BAR();
      float ml;
      { const float v = ((LAS float*)(L + L_WM))[lane & 15]; ml = max_lo3(v); }
      BAR();
      f4 s = {0.f, 0.f, 0.f, 0.f};
#pragma unroll
      for (int w = 0; w < 8; ++w) s += *(LAS f4*)(L + L_OW + w * 1024 + lane * 16);
      { const float l = sum_lo3(((LAS float*)(L + L_WL))[lane & 15]);
        if ((lane & 55) == 0) st8(rs, WS_PO + (kvh * 8 + sp) * PO_REC + 1024 + lane, v2u{nosent(ml), nosent(l)}); }
      st16(rs, WS_PO + (kvh * 8 + sp) * PO_REC + lane * 16, v4u{nosent(s.x), nosent(s.y), nosent(s.z), nosent(s.w)});
      STAMP(3);
      poll_tag(rs, WS_QG + lane * 256, tag);
      BAR();
      publish_ap(rs, L, kvh, sp, lane);
      STAMP(4);
    }
  } else if (j < 31) {
    const int kvh = 2 * xc + (j - 29);
    if (!helper) {
      v4u wv[4];
      load_wo(p, wv, kvh, 8, wave, lane);
      PIN();
      BAR();
      o_stage(rs, L, wv, kvh, wave, lane);
      BAR();
    } else {
      tag = make_tag(ld8(rs, WS_EP));
      poll_tag(rs, WS_QG + (kvh * 64 + lane) * 16, tag);
      BAR();
      BAR();
      publish_ap(rs, L, kvh, 8, lane);
      STAMP(4);
    }
    FLUSH();
    return;
  } else {
    return;
  }

  {
    const int col = 64 * wave + 4 * (lane & 15) + (lane >> 4), d0 = 3 * fi;
    const unsigned xb = ldh(p.x + col);
    v4u w1v, b1v, w2v; unsigned w1e, b1e, w2e;
    const int tw = threadIdx.x;
    if (!helper) {
      w1v = ldg(p.w1 + 8 * tw); b1v = ldg(p.b1 + 8 * tw); w2v = ldg(p.w2 + 8 * tw);
      w1e = ldh(p.w1 + 4096 + tw); b1e = ldh(p.b1 + 4096 + tw); w2e = ldh(p.w2 + 4096 + tw);
      PIN();
    }
    const unsigned b2b = ldh(p.b2);
    PIN();
    float ga, sb, av, gb;
    {
      const int off = WS_MG + (16 * wave + (lane & 15)) * 16;
      v4u g0, g1, g2, g3;
      poll_tag4(rs, off, 144 * 16, tag, g0, g1, g2, g3);
      const bool hi = lane & 32; const int sh = (lane & 16) ? 0 : 16;
      ga = __uint_as_float(((hi ? g0.z : g0.x) << sh) & 0xFFFF0000u);
      sb = __uint_as_float(((hi ? g1.z : g1.x) << sh) & 0xFFFF0000u);
      av = rb(1.f + __uint_as_float(((hi ? g2.z : g2.x) << sh) & 0xFFFF0000u));
      gb = __uint_as_float(((hi ? g3.z : g3.x) << sh) & 0xFFFF0000u);
    }
    STAMP(5);
    {
      const float v[8] = {av, av * av, av * sb, sb, sb * sb, 0.f, 0.f, 0.f};
      const float r = sum_lo3(red8_hi(v, lane));
      if ((lane & 7) == 0) ((LAS float*)(L + L_ST0))[wave * 8 + (lane >> 3)] = r;
      const unsigned k = (unsigned)(col - d0);
      if (k < 3u) { LAS float* own = (LAS float*)(L + L_OWN); own[4 + k] = av; own[8 + k] = sb; own[12 + k] = gb; }
    }
    f2 A0[5], A1[5], A2[5], A3[5], U0[5], U1[5];
    if (!helper) {
      asm volatile("" : "+v"(w1e), "+v"(b1e), "+v"(w2e));
      const float c1 = -2.3022081986f, c2 = -0.1029432397f;
#pragma unroll
      for (int i = 0; i < 5; ++i) {
        const unsigned a = i < 4 ? w1v[i] : w1e, b = i < 4 ? b1v[i] : b1e, c = i < 4 ? w2v[i] : w2e;
        const f2 wa = {bf2f(a), __uint_as_float(a & 0xFFFF0000u)}, ba = {bf2f(b), __uint_as_float(b & 0xFFFF0000u)}, wb = {bf2f(c), __uint_as_float(c & 0xFFFF0000u)};
        const f2 cw = c2 * wa * wa, cbb = c2 * ba * ba;
        A3[i] = cw * wa; A2[i] = 3.f * cw * ba; A1[i] = wa * (c1 + 3.f * cbb); A0[i] = ba * (c1 + cbb);
        U1[i] = wa * wb; U0[i] = ba * wb;
        asm volatile("" : "+v"(A0[i]), "+v"(A1[i]), "+v"(A2[i]), "+v"(A3[i]), "+v"(U0[i]), "+v"(U1[i]));
      }
    }
    BAR();
    float sA, sAA, sAS, sS, sSS;
    {
      float r = ((LAS float*)(L + L_ST0))[(lane & 7) * 8 + (lane >> 3)];
      r = sum_lo3(r) + ((LAS float*)(L + L_ST0))[64 + (lane >> 3)];
      sA = rdl(r, 0); sAA = rdl(r, 8); sAS = rdl(r, 16); sS = rdl(r, 24); sSS = rdl(r, 32);
    }
    const float av_d = ((LAS float*)(L + L_OWN))[4 + (lane & 3)], sb_d = ((LAS float*)(L + L_OWN))[8 + (lane & 3)];
    STAMP(6);
    float x1;
    {
      const int off = WS_AP + (wave * 16 + (lane >> 4)) * 256 + (lane & 15) * 16;
      v4u a0 = ld16(rs, off), a1 = ld16(rs, off + 1024), a2 = ld16(rs, off + 2048), a3 = ld16(rs, off + 3072);
      for (int s = 0; s < SPIN_CAP; ++s) {
        const bool k0 = __any(has_sent(a0)), k1 = __any(has_sent(a1)), k2 = __any(has_sent(a2)), k3 = __any(has_sent(a3));
        if (!(k0 | k1 | k2 | k3)) break;
        asm volatile("" ::: "memory");
        if (k0) a0 = ld16(rs, off);
        if (k1) a1 = ld16(rs, off + 1024);
        if (k2) a2 = ld16(rs, off + 2048);
        if (k3) a3 = ld16(rs, off + 3072);
      }
      STAMP(7);
      const f4 s = (__builtin_bit_cast(f4, a0) + __builtin_bit_cast(f4, a1)) + (__builtin_bit_cast(f4, a2) + __builtin_bit_cast(f4, a3));
      const float at = sw16(sw32(s.x, s.z), sw32(s.y, s.w));
      x1 = rb(bf2f(xb) + rb(ga * rb(at)));
      const float uu = x1 * av;
      const float v[8] = {x1, x1 * x1, uu, uu * uu, uu * av, uu * sb, 0.f, 0.f};
      const float r = sum_lo3(red8_hi(v, lane));
      if ((lane & 7) == 0) ((LAS float*)(L + L_ST1))[wave * 8 + (lane >> 3)] = r;
      const unsigned k = (unsigned)(col - d0);
      if (k < 3u) ((LAS float*)(L + L_OWN))[k] = x1;
    }
    BAR();
    const float x1_d = ((LAS float*)(L + L_OWN))[lane & 3];
    if (!helper) {
      float r = ((LAS float*)(L + L_ST1))[(lane & 7) * 8 + (lane >> 3)];
      r = sum_lo3(r) + ((LAS float*)(L + L_ST1))[64 + (lane >> 3)];
      const float sX = rdl(r, 0), sXX = rdl(r, 8), sU = rdl(r, 16), sUU = rdl(r, 24), sUA = rdl(r, 32), sUS = rdl(r, 40);
      const float mean = sX * (1.f / D), var = fmaxf(sXX * (1.f / D) - mean * mean, 0.f);
      const float m1 = rb(mean), r1 = __builtin_amdgcn_rcpf(rb(__builtin_amdgcn_sqrtf(rb(rb(var) + 1e-5f))));
      const float mu = (r1 * (sU - m1 * sA) + sS) * (1.f / D);
      const float e2 = (r1 * r1 * (sUU - 2.f * m1 * sUA + m1 * m1 * sAA) + 2.f * r1 * (sUS - m1 * sAS) + sSS) * (1.f / D);
      const float var2 = fmaxf(e2 - mu * mu, 0.f);
      const float m2 = rb(mu), r2 = __builtin_amdgcn_rcpf(rb(__builtin_amdgcn_sqrtf(rb(rb(var2) + 1e-5f))));
      const float z = rb(rb(x1_d - m1) * r1);
      const float h = rb(rb(z * av_d) + sb_d);
      const float t = rb(rb(h - m2) * r2);
      STAMP(8);
      const float t0 = rdl(t, 0), t1 = rdl(t, 1), t2 = rdl(t, 2);
      f2 acc[3];
#pragma unroll
      for (int d = 0; d < 3; ++d) {
        const float td = d == 0 ? t0 : d == 1 ? t1 : t2;
        const f2 tt = {td, td};
        acc[d] = f2{0.f, 0.f};
#pragma unroll
        for (int i = 0; i < 4; ++i) acc[d] = gelu2(A3[i], A2[i], A1[i], A0[i], U1[i], U0[i], tt, acc[d]);
      }
      const f2 e01 = gelu2(f2{A3[4].x, A3[4].x}, f2{A2[4].x, A2[4].x}, f2{A1[4].x, A1[4].x}, f2{A0[4].x, A0[4].x}, f2{U1[4].x, U1[4].x}, f2{U0[4].x, U0[4].x}, f2{t0, t1}, f2{acc[0].x + acc[0].y, acc[1].x + acc[1].y});
      const float a2 = fmaf(fmaf(fmaf(A3[4].x, t2, A2[4].x), t2, A1[4].x), t2, A0[4].x);
      const float e2s = fmaf(fmaf(U1[4].x, t2, U0[4].x), __builtin_amdgcn_rcpf(1.f + __builtin_amdgcn_exp2f(a2)), acc[2].x + acc[2].y);
      float q = sw16(sw32(e01.x, e2s), sw32(e01.y, 0.f));
      q += dpp<R8>(q); q = sum_lo3(q);
      if ((lane & 15) == 0) ((LAS float*)(L + L_FIN))[wave * 4 + (lane >> 4)] = q;
    }
    STAMP(9);
    BAR();
    if (helper) {
      if (lane < 3) {
        float y = 0.f;
#pragma unroll
        for (int w = 0; w < 8; ++w) y += ((LAS float*)(L + L_FIN))[w * 4 + lane];
        y = rb(rb(y) + bf2f(b2b));
        const float gbd = ((LAS float*)(L + L_OWN))[12 + lane];
        *(GAS u16*)(p.out + d0 + lane) = (u16)(__float_as_uint(rb(x1_d + rb(gbd * y))) >> 16);
      }
      if (fi == 0 && lane == 0) st8(rs, WS_EP, tag);
      STAMP(10);
    }
    FLUSH();
  }
}

extern "C" void kernel_launch(void* const* d_in, const int* in_sizes, int n_in, void* d_out, int out_size, void* d_ws, size_t ws_size, hipStream_t stream) {
  Params p;
  p.x = (const u16*)d_in[0]; p.kc = (const u16*)d_in[1]; p.vc = (const u16*)d_in[2]; p.wq = (const u16*)d_in[3]; p.wo = (const u16*)d_in[4];
  p.w1 = (const u16*)d_in[6]; p.b1 = (const u16*)d_in[7]; p.w2 = (const u16*)d_in[8]; p.b2 = (const u16*)d_in[9];
  p.c = (const u16*)d_in[10]; p.wm = (const u16*)d_in[11];
  p.out = (u16*)d_out; p.ws = (unsigned char*)d_ws;
  fused_k<<<NWG, NTHR, 0, stream>>>(p);
}
```

```cpp
#define PROBE 99
#define PROBE_ROLE (true)
#include <hip/hip_runtime.h>
#include <stdint.h>

typedef unsigned short u16;
typedef unsigned long long u64;
typedef unsigned v4u __attribute__((ext_vector_type(4)));
typedef unsigned v2u __attribute__((ext_vector_type(2)));
typedef float f4 __attribute__((ext_vector_type(4)));
typedef float f2 __attribute__((ext_vector_type(2)));
typedef __bf16 bf2 __attribute__((ext_vector_type(2)));
typedef __bf16 bf8 __attribute__((ext_vector_type(8)));
#define GAS __attribute__((address_space(1)))
#define LAS __attribute__((address_space(3)))

constexpr int D = 576, NMOD = 6 * D, SK = 8192, DH = 128, W = 2048, F = 4608, NQ = 4096;
constexpr int NWG = 256, NTHR = 576;
constexpr int WS_EP = 0;
constexpr int WS_HG = 256;
constexpr int WS_QG = WS_HG + 4 * 288 * 16;
constexpr int WS_MG = WS_QG + 1024 * 16;
constexpr int WS_AP = WS_MG + 576 * 16;
constexpr int PO_REC = 1152;
constexpr int WS_PO = WS_AP + 9 * 16 * 256;
constexpr int WS_END = WS_PO + 16 * 8 * PO_REC;
constexpr int CODE_BYTES = 17152;
constexpr unsigned SENT = 0xFFFFFFFFu;
constexpr int SPIN_CAP = 1 << 17;
constexpr int AUX_SC1 = 16;

#define STAMP(i) do { if ((i) == 0 || (i) == PROBE) { PIN(); tsv[(i) ? 1 : 0] = __builtin_amdgcn_s_memrealtime(); PIN(); } } while (0)
#define STAMP_DECL u64 tsv[2] = {0, 0}
#define FLUSH() do { if (PROBE_ROLE) { const u64 lim = tsv[0] + 3000 + (tsv[1] ? 2 * (tsv[1] - tsv[0]) : 0); while (__builtin_amdgcn_s_memrealtime() < lim) __builtin_amdgcn_s_sleep(1); } } while (0)

struct Params {
  const u16 *x, *kc, *vc, *wq, *wo, *w1, *b1, *w2, *b2, *c, *wm;
  u16* out;
  unsigned char* ws;
};

constexpr int L_K = 0;
constexpr int L_RED = 65536;
constexpr int L_HT = L_RED + 2048;
constexpr int L_QS = L_HT + 2048;
constexpr int L_WM = L_QS + 512;
constexpr int L_WL = L_WM + 64;
constexpr int L_OW = L_WL + 64;
constexpr int L_ST0 = L_OW + 8192;
constexpr int L_ST1 = L_ST0 + 320;
constexpr int L_OWN = L_ST1 + 320;
constexpr int L_FIN = L_OWN + 64;
constexpr int L_TAG = L_FIN + 128;
constexpr int L_END = L_TAG + 16;
constexpr int L_BYTES = 96 * 1024;
static_assert(L_END <= L_BYTES, "lds");

__device__ __forceinline__ float bf2f(unsigned h) { return __uint_as_float(h << 16); }
__device__ __forceinline__ float rb(float x) { f2 v = {0.f, x}; return __builtin_bit_cast(float, __builtin_convertvector(v, bf2)); }
__device__ __forceinline__ unsigned pk2(float lo, float hi) { f2 v = {lo, hi}; return __builtin_bit_cast(unsigned, __builtin_convertvector(v, bf2)); }
__device__ __forceinline__ float dot2(unsigned a, unsigned b, float c) { return __builtin_amdgcn_fdot2_f32_bf16(__builtin_bit_cast(bf2, a), __builtin_bit_cast(bf2, b), c, false); }
template <int C> __device__ __forceinline__ unsigned dppu(unsigned x) { return (unsigned)__builtin_amdgcn_mov_dpp((int)x, C, 0xf, 0xf, true); }
template <int C> __device__ __forceinline__ float dpp(float x) { return __uint_as_float(dppu<C>(__float_as_uint(x))); }
constexpr int X1 = 0xB1, X2 = 0x4E, X7 = 0x141, R4 = 0x124, R8 = 0x128;
__device__ __forceinline__ float sw32(float a, float b) { auto r = __builtin_amdgcn_permlane32_swap(__float_as_uint(a), __float_as_uint(b), false, false); return __uint_as_float(r[0]) + __uint_as_float(r[1]); }
__device__ __forceinline__ float sw16(float a, float b) { auto r = __builtin_amdgcn_permlane16_swap(__float_as_uint(a), __float_as_uint(b), false, false); return __uint_as_float(r[0]) + __uint_as_float(r[1]); }
__device__ __forceinline__ float mx32(float a) { auto r = __builtin_amdgcn_permlane32_swap(__float_as_uint(a), __float_as_uint(a), false, false); return fmaxf(__uint_as_float(r[0]), __uint_as_float(r[1])); }
__device__ __forceinline__ float mx16(float a) { auto r = __builtin_amdgcn_permlane16_swap(__float_as_uint(a), __float_as_uint(a), false, false); return fmaxf(__uint_as_float(r[0]), __uint_as_float(r[1])); }
__device__ __forceinline__ float red8_hi(const float (&a)[8], int lane) {
  float v0 = sw32(a[0], a[4]), v1 = sw32(a[1], a[5]), v2 = sw32(a[2], a[6]), v3 = sw32(a[3], a[7]);
  float u0 = sw16(v0, v2), u1 = sw16(v1, v3);
  float t0 = u0 + dpp<R8>(u0), t1 = u1 + dpp<R8>(u1);
  return (lane & 8) ? t1 : t0;
}
__device__ __forceinline__ float sum_lo3(float v) { v += dpp<X1>(v); v += dpp<X2>(v); v += dpp<X7>(v); return v; }
__device__ __forceinline__ float max_lo3(float v) { v = fmaxf(v, dpp<X1>(v)); v = fmaxf(v, dpp<X2>(v)); v = fmaxf(v, dpp<X7>(v)); return v; }
__device__ __forceinline__ unsigned nosent(float v) { unsigned u = __float_as_uint(v); return u == SENT ? 0x7FC00000u : u; }
__device__ __forceinline__ v4u ldnt(const void* p) { return __builtin_nontemporal_load((const GAS v4u*)p); }
__device__ __forceinline__ v4u ldg(const void* p) { return *(const GAS v4u*)p; }
__device__ __forceinline__ unsigned ldh(const u16* p) { return *(const GAS u16*)p; }
#define BAR() do { asm volatile("s_waitcnt lgkmcnt(0)" ::: "memory"); __builtin_amdgcn_s_barrier(); asm volatile("" ::: "memory"); } while (0)
#define PIN() __builtin_amdgcn_sched_barrier(0)
typedef __amdgpu_buffer_rsrc_t rsrc_t;
__device__ __forceinline__ v4u ld16(rsrc_t r, int off) { return __builtin_amdgcn_raw_buffer_load_b128(r, off, 0, AUX_SC1); }
__device__ __forceinline__ v2u ld8(rsrc_t r, int off) { return __builtin_amdgcn_raw_buffer_load_b64(r, off, 0, AUX_SC1); }
__device__ __forceinline__ void st16(rsrc_t r, int off, v4u v) { __builtin_amdgcn_raw_buffer_store_b128(v, r, off, 0, AUX_SC1); }
__device__ __forceinline__ void st8(rsrc_t r, int off, v2u v) { __builtin_amdgcn_raw_buffer_store_b64(v, r, off, 0, AUX_SC1); }
__device__ __forceinline__ void st4(rsrc_t r, int off, unsigned v) { __builtin_amdgcn_raw_buffer_store_b32(v, r, off, 0, AUX_SC1); }
__device__ __forceinline__ v2u make_tag(v2u e) {
  return v2u{__builtin_amdgcn_readfirstlane(e.x) + 0x9E3779B9u, __builtin_amdgcn_readfirstlane(e.y) + 0x7F4A7C15u};
}
__device__ __forceinline__ v4u poll_tag(rsrc_t r, int off, v2u tag) {
  v4u g;
  for (int s = 0; s < SPIN_CAP; ++s) {
    asm volatile("" ::: "memory");
    g = ld16(r, off);
    if (__all(g.y == tag.x && g.w == tag.y)) break;
  }
  return g;
}

__device__ __forceinline__ float rdl(float v, int l) { return __uint_as_float(__builtin_amdgcn_readlane(__float_as_uint(v), l)); }
__device__ __forceinline__ bool has_sent(v4u a) { return max(max(a.x, a.y), max(a.z, a.w)) == SENT; }
__device__ __forceinline__ bool tag_bad(v4u g, v2u tag) { return (g.y != tag.x) | (g.w != tag.y); }
__device__ __forceinline__ f2 gelu2(f2 A3, f2 A2, f2 A1, f2 A0, f2 U1, f2 U0, f2 t, f2 acc) {
  const f2 a = __builtin_elementwise_fma(__builtin_elementwise_fma(__builtin_elementwise_fma(A3, t, A2), t, A1), t, A0);
  const f2 d = f2{__builtin_amdgcn_exp2f(a.x), __builtin_amdgcn_exp2f(a.y)} + 1.f;
  return __builtin_elementwise_fma(__builtin_elementwise_fma(U1, t, U0), f2{__builtin_amdgcn_rcpf(d.x), __builtin_amdgcn_rcpf(d.y)}, acc);
}
__device__ __forceinline__ void poll_tag4(rsrc_t r, int off, int stride, v2u tag, v4u& g0, v4u& g1, v4u& g2, v4u& g3) {
  g0 = ld16(r, off); g1 = ld16(r, off + stride); g2 = ld16(r, off + 2 * stride); g3 = ld16(r, off + 3 * stride);
  for (int s = 0; s < SPIN_CAP; ++s) {
    const bool k0 = __any(tag_bad(g0, tag)), k1 = __any(tag_bad(g1, tag)), k2 = __any(tag_bad(g2, tag)), k3 = __any(tag_bad(g3, tag));
    if (!(k0 | k1 | k2 | k3)) break;
    asm volatile("" ::: "memory");
    if (k0) g0 = ld16(r, off);
    if (k1) g1 = ld16(r, off + stride);
    if (k2) g2 = ld16(r, off + 2 * stride);
    if (k3) g3 = ld16(r, off + 3 * stride);
  }
}

__device__ __forceinline__ void row_fma(float (&acc)[8], v4u w, unsigned slo) {
  const unsigned shi = slo << 16;
#pragma unroll
  for (int k = 0; k < 4; ++k) { acc[2 * k] = dot2(w[k], slo, acc[2 * k]); acc[2 * k + 1] = dot2(w[k], shi, acc[2 * k + 1]); }
}
__device__ __forceinline__ void pair_rows(v4u& a, v4u& b) {
#pragma unroll
  for (int k = 0; k < 4; ++k) { const unsigned lo = __builtin_amdgcn_perm(b[k], a[k], 0x05040100u), hi = __builtin_amdgcn_perm(b[k], a[k], 0x07060302u); a[k] = lo; b[k] = hi; }
}
__device__ __forceinline__ void pair_fma(float (&acc)[8], v4u lo, v4u hi, unsigned s2) {
#pragma unroll
  for (int k = 0; k < 4; ++k) { acc[2 * k] = dot2(lo[k], s2, acc[2 * k]); acc[2 * k + 1] = dot2(hi[k], s2, acc[2 * k + 1]); }
}
__device__ __forceinline__ unsigned silu_bits(unsigned cb) {
  const float c = bf2f(cb);
  return __float_as_uint(rb(c * __builtin_amdgcn_rcpf(1.f + __builtin_amdgcn_exp2f(c * -1.4426950408889634f)))) >> 16;
}

__device__ __forceinline__ float ln_input(LAS unsigned char* L, float xv, int wave, int lane) {
  float v = sw32(xv, xv * xv); v += dpp<R8>(v); v = sw16(v, v); v = sum_lo3(v);
  if ((lane & 31) == 0) ((LAS float*)(L + L_ST0))[wave * 8 + (lane >> 5)] = v;
  BAR();
  float s1 = 0.f, s2 = 0.f;
#pragma unroll
  for (int w = 0; w < 9; ++w) { const f2 u = *(LAS f2*)(L + L_ST0 + w * 32); s1 += u.x; s2 += u.y; }
  const float mean = s1 * (1.f / D), var = fmaxf(s2 * (1.f / D) - mean * mean, 0.f);
  const float sq = rb(__builtin_amdgcn_sqrtf(rb(rb(var) + 1e-5f)));
  return rb(rb(xv - rb(mean)) * __builtin_amdgcn_rcpf(sq));
}

__device__ __forceinline__ void o_stage(rsrc_t rs, LAS unsigned char* L, v4u (&wv)[4], int kvh, int wave, int lane) {
  pair_rows(wv[0], wv[1]); pair_rows(wv[2], wv[3]);
  const int hd = wave >> 2, sp = lane & 7;
  const int offo = WS_PO + (kvh * 8 + sp) * PO_REC + hd * 512 + (wave & 3) * 128 + (lane >> 3) * 16;
  const int offm = WS_PO + (kvh * 8 + sp) * PO_REC + 1024 + hd * 8;
  v4u po; v2u ml;
  for (int s = 0; s < SPIN_CAP; ++s) {
    asm volatile("" ::: "memory");
    po = ld16(rs, offo); ml = ld8(rs, offm);
    if (__all(max(max(max(po.x, po.y), max(po.z, po.w)), max(ml.x, ml.y)) != SENT)) break;
  }
  const float m = __uint_as_float(ml.x), M = max_lo3(m);
  const float e = __builtin_amdgcn_exp2f((m - M) * 1.4426950408889634f);
  const float Ls = sum_lo3(e * __uint_as_float(ml.y));
  const float wgt = e * __builtin_amdgcn_rcpf(Ls);
  float o[4];
#pragma unroll
  for (int k = 0; k < 4; ++k) o[k] = sum_lo3(wgt * __uint_as_float(po[k]));
  float acc[8] = {0, 0, 0, 0, 0, 0, 0, 0};
  pair_fma(acc, wv[0], wv[1], pk2(o[0], o[1])); pair_fma(acc, wv[2], wv[3], pk2(o[2], o[3]));
  const float r = red8_hi(acc, lane);
  ((LAS float*)(L + L_RED))[wave * 64 + 8 * (lane & 7) + (lane >> 3)] = r;
}
__device__ __forceinline__ void load_wo(const Params& p, v4u (&wv)[4], int kvh, int cb, int wave, int lane) {
  const u16* src = p.wo + (size_t)((2 * kvh + (wave >> 2)) * DH + 32 * (wave & 3) + 4 * (lane >> 3)) * D + 64 * cb + 8 * (lane & 7);
#pragma unroll
  for (int i = 0; i < 4; ++i) wv[i] = ldnt(src + i * D);
}
__device__ __forceinline__ void publish_ap(rsrc_t rs, LAS unsigned char* L, int kvh, int cb, int lane) {
  float s = 0.f;
#pragma unroll
  for (int w = 0; w < 8; ++w) s += ((LAS float*)(L + L_RED))[w * 64 + lane];
  st4(rs, WS_AP + (cb * 16 + kvh) * 256 + lane * 4, nosent(s));
}

__global__ void __launch_bounds__(NTHR) fused_k(Params p) {
  __shared__ __attribute__((aligned(16))) unsigned char smem[L_BYTES];
  LAS unsigned char* L = (LAS unsigned char*)smem;
  u64 pc0;
  asm volatile("s_getpc_b64 %0" : "=s"(pc0));
  const int lane = threadIdx.x & 63, wave = __builtin_amdgcn_readfirstlane(threadIdx.x >> 6);
  const int xc = blockIdx.x & 7, j = blockIdx.x >> 3;
  const bool helper = wave == 8;
  const rsrc_t rs = __builtin_amdgcn_make_buffer_rsrc((void*)p.ws, 0, WS_END, 0x00020000);
  STAMP_DECL;
  STAMP(0);
  int fi;
  v2u tag;
  if (j == 31) {
    const unsigned char* pc = (const unsigned char*)(pc0 & ~127ull);
    const int o = threadIdx.x * 16;
    v4u a = ldg(pc + o), b = ldg(pc + (o + NTHR * 16 < CODE_BYTES ? o + NTHR * 16 : o));
    asm volatile("" :: "v"(a), "v"(b));
    if (wave == 0) asm volatile("buffer_wbl2 sc1\n\ts_waitcnt vmcnt(0)" ::: "memory");
    return;
  }
  if (j < 5) {
    const int u = xc + 8 * j;
    if (u >= 36) return;
    const int g = lane >> 3, ch = lane & 7;
    if (!helper) {
      const int cb = u % 18, r0 = 288 * (u / 18) + 36 * wave + g;
      unsigned cv[5]; v4u wv[5];
#pragma unroll
      for (int i = 0; i < 4; ++i) cv[i] = ldh(p.c + r0 + 8 * i);
      cv[4] = ldh(p.c + r0 + (g < 4 ? 32 : 0));
      const u16* src = p.wm + (size_t)r0 * NMOD + 64 * cb + 8 * ch;
#pragma unroll
      for (int i = 0; i < 4; ++i) wv[i] = ldnt(src + (size_t)8 * i * NMOD);
      { const rsrc_t rw = __builtin_amdgcn_make_buffer_rsrc((void*)p.wm, 0, D * NMOD * 2, 0x00020000);
        wv[4] = __builtin_amdgcn_raw_buffer_load_b128(rw, g < 4 ? ((r0 + 32) * NMOD + 64 * cb + 8 * ch) * 2 : 0x7FFFFFF0, 0, 2); }
      PIN();
      float acc[8] = {0, 0, 0, 0, 0, 0, 0, 0};
#pragma unroll
      for (int i = 0; i < 5; ++i) cv[i] = silu_bits(cv[i]);
#pragma unroll
      for (int i = 0; i < 5; ++i) row_fma(acc, wv[i], cv[i]);
      STAMP(1);
      ((LAS float*)(L + L_RED))[wave * 64 + 8 * ch + g] = red8_hi(acc, lane);
      BAR();
      unsigned c2[9];
#pragma unroll
      for (int i = 0; i < 9; ++i) c2[i] = ldh(p.c + 72 * wave + 8 * i + g);
#pragma unroll
      for (int i = 0; i < 9; ++i) { c2[i] = silu_bits(c2[i]); asm volatile("" : "+v"(c2[i])); }
      BAR();
      const u16* s2 = p.wm + (size_t)(72 * wave + g) * NMOD + 64 * (18 + u) + 8 * ch;
      v4u w2[9];
#pragma unroll
      for (int i = 0; i < 9; ++i) w2[i] = ldnt(s2 + (size_t)8 * i * NMOD);
      PIN();
#pragma unroll
      for (int k = 0; k < 8; ++k) acc[k] = 0.f;
#pragma unroll
      for (int i = 0; i < 9; ++i) row_fma(acc, w2[i], c2[i]);
      ((LAS float*)(L + L_RED))[wave * 64 + 8 * ch + g] = red8_hi(acc, lane);
      STAMP(3);
      BAR();
    } else {
      tag = make_tag(ld8(rs, WS_EP));
      BAR();
      if (lane < 32) {
        f2 s = {0.f, 0.f};
#pragma unroll
        for (int w = 0; w < 8; ++w) s += *(LAS f2*)(L + L_RED + w * 256 + lane * 8);
        const int cb = u % 18;
        st16(rs, WS_HG + (((cb / 9) * 2 + u / 18) * 288 + (cb % 9) * 32 + lane) * 16, v4u{__float_as_uint(s.x), tag.x, __float_as_uint(s.y), tag.y});
      }
      STAMP(2);
      poll_tag(rs, WS_QG + ((u * 7) & 63) * 256, tag);
      BAR();
      BAR();
      if (lane < 16) {
        f4 s = {0.f, 0.f, 0.f, 0.f};
#pragma unroll
        for (int w = 0; w < 8; ++w) s += *(LAS f4*)(L + L_RED + w * 256 + lane * 16);
        st16(rs, WS_MG + (u * 16 + lane) * 16, v4u{pk2(s.x, s.y), tag.x, pk2(s.z, s.w), tag.y});
      }
      STAMP(4);
    }
    FLUSH();
    return;
  } else if (j < 13) {
    const int qb = 8 * xc + (j - 5), kvh = qb >> 2, part = qb & 3, t = threadIdx.x;
    const int g = lane >> 3, ch = lane & 7;
    fi = 128 + qb;
    v2u ep = ld8(rs, WS_EP);
    unsigned xb = ldh(p.x + t);
    v4u wv[9];
    float ln;
    if (!helper) {
      const u16* src = p.wq + (size_t)(72 * wave + g) * NQ + 64 * qb + 8 * ch;
#pragma unroll
      for (int i = 0; i < 9; ++i) wv[i] = ldnt(src + (size_t)8 * i * NQ);
      PIN();
      asm volatile("" : "+v"(ep), "+v"(xb));
      tag = make_tag(ep);
      ln = ln_input(L, bf2f(xb), wave, lane);
      pair_rows(wv[0], wv[1]); pair_rows(wv[2], wv[3]); pair_rows(wv[4], wv[5]); pair_rows(wv[6], wv[7]); asm volatile("" : "+v"(wv[8]));
    } else {
      const v4u sv = {SENT, SENT, SENT, SENT};
      const int id = part * 36 + lane;
      const int rec = WS_PO + (kvh * 8 + part * 2) * PO_REC;
      st16(rs, rec + lane * 16, sv);
      st16(rs, rec + PO_REC + lane * 16, sv);
      st16(rs, lane < 36 ? WS_AP + ((id >> 4) * 16 + kvh) * 256 + (id & 15) * 16 : lane < 38 ? rec + (lane - 36) * PO_REC + 1024 : 0x7FFFFFF0, sv);
      PIN();
      asm volatile("" : "+v"(ep), "+v"(xb));
      tag = make_tag(ep);
      ln = ln_input(L, bf2f(xb), wave, lane);
    }
    STAMP(1);
    {
      const int off = WS_HG + (t >> 1) * 16;
      v4u g0, g1, g2, g3;
      poll_tag4(rs, off, 288 * 16, tag, g0, g1, g2, g3);
      const bool odd = t & 1;
      const float sa = rb(__uint_as_float(odd ? g0.z : g0.x) + __uint_as_float(odd ? g1.z : g1.x));
      const float ca = rb(__uint_as_float(odd ? g2.z : g2.x) + __uint_as_float(odd ? g3.z : g3.x));
      const float h = rb(rb(ln * rb(1.f + ca)) + sa);
      const int hw = t / 72, rem = t - 72 * hw;
      *(LAS u16*)(L + L_HT + (hw * 8 + (rem & 7)) * 32 + (rem >> 3) * 2) = (u16)(__float_as_uint(h) >> 16);
    }
    STAMP(2);
    BAR();
    if (!helper) {
      const v4u hp = *(LAS v4u*)(L + L_HT + (wave * 8 + g) * 32);
      const unsigned h8 = *(LAS u16*)(L + L_HT + (wave * 8 + g) * 32 + 16);
      float acc[8] = {0, 0, 0, 0, 0, 0, 0, 0};
      pair_fma(acc, wv[0], wv[1], hp.x); pair_fma(acc, wv[2], wv[3], hp.y); pair_fma(acc, wv[4], wv[5], hp.z); pair_fma(acc, wv[6], wv[7], hp.w);
      row_fma(acc, wv[8], h8);
      ((LAS float*)(L + L_RED))[wave * 64 + 8 * ch + g] = red8_hi(acc, lane);
    }
    BAR();
    if (helper) {
      f4 s = {0.f, 0.f, 0.f, 0.f};
#pragma unroll
      for (int w = 0; w < 8; ++w) s += *(LAS f4*)(L + L_RED + w * 256 + (lane & 15) * 16);
      asm volatile("s_waitcnt vmcnt(0)" ::: "memory");
      if (lane < 16) st16(rs, WS_QG + (qb * 16 + lane) * 16, v4u{pk2(s.x, s.y), tag.x, pk2(s.z, s.w), tag.y});
      STAMP(3);
      poll_tag(rs, WS_QG + lane * 256, tag);
      STAMP(4);
    }
  } else if (j < 29) {
    const int kvh = 2 * xc + ((j - 13) >> 3), sp = (j - 13) & 7;
    fi = kvh * 8 + sp;
    if (!helper) {
      const int g = lane >> 4, c = lane & 15;
      const size_t kb = ((size_t)kvh * SK + (SK - W) + 256 * sp + 32 * wave) * DH;
      LAS unsigned char* kt = L + L_K + wave * 8192;
      {
        const unsigned vo = g * 256 + ((c ^ g) << 4), l0 = (unsigned)(uintptr_t)kt;
        const u16* kp = p.kc + kb;
        unsigned keep;
        asm volatile("s_mov_b32 %0, m0\n\ts_mov_b32 m0, %5\n\ts_nop 4\n\t"
                     "global_load_lds_dwordx4 %1, %7 nt\n\tglobal_load_lds_dwordx4 %2, %7 offset:1024 nt\n\t"
                     "global_load_lds_dwordx4 %3, %7 offset:2048 nt\n\tglobal_load_lds_dwordx4 %4, %7 offset:3072 nt\n\t"
                     "s_mov_b32 m0, %6\n\ts_nop 0\n\t"
                     "global_load_lds_dwordx4 %1, %8 nt\n\tglobal_load_lds_dwordx4 %2, %8 offset:1024 nt\n\t"
                     "global_load_lds_dwordx4 %3, %8 offset:2048 nt\n\tglobal_load_lds_dwordx4 %4, %8 offset:3072 nt\n\t"
                     "s_mov_b32 m0, %0"
                     : "=&s"(keep) : "v"(vo), "v"(vo ^ 64u), "v"(vo ^ 128u), "v"(vo ^ 192u), "s"(l0), "s"(l0 + 4096u), "s"(kp), "s"(kp + 2048) : "memory");
      }
      v4u vv[8], wv[4];
#pragma unroll
      for (int i = 0; i < 8; ++i) vv[i] = ldnt(p.vc + kb + (16 * (i >> 2) + 4 * g + (i & 3)) * DH + 8 * c);
      load_wo(p, wv, kvh, sp, wave, lane);
      PIN();
      asm volatile("s_waitcnt vmcnt(12)" ::: "memory");
      v4u kf[8];
#pragma unroll
      for (int T = 0; T < 2; ++T)
#pragma unroll
        for (int s = 0; s < 4; ++s) kf[T * 4 + s] = *(LAS v4u*)(kt + (16 * T + c) * 256 + ((4 * s + g) ^ c) * 16);
      STAMP(1);
      BAR();
      { const v2u tg = *(LAS v2u*)(L + L_TAG); tag.x = __builtin_amdgcn_readfirstlane(tg.x); tag.y = __builtin_amdgcn_readfirstlane(tg.y); }
      f4 d0 = {0, 0, 0, 0}, d1 = {0, 0, 0, 0};
#pragma unroll
      for (int s = 0; s < 4; ++s) {
        const v4u qf = *(LAS v4u*)(L + L_QS + (c & 1) * 256 + (4 * s + g) * 16);
        d0 = __builtin_amdgcn_mfma_f32_16x16x32_bf16(__builtin_bit_cast(bf8, kf[s]), __builtin_bit_cast(bf8, qf), d0, 0, 0, 0);
        d1 = __builtin_amdgcn_mfma_f32_16x16x32_bf16(__builtin_bit_cast(bf8, kf[4 + s]), __builtin_bit_cast(bf8, qf), d1, 0, 0, 0);
      }
      const f4 ds = (c & 8) ? d1 : d0;
      const float e0 = (c & 4) ? ds.z : ds.x, e1 = (c & 4) ? ds.w : ds.y;
      const float sc = rb(rb((c & 2) ? e1 : e0) * 0.08838834764831845f);
      float m = fmaxf(sc, dpp<X2>(sc)); m = fmaxf(m, dpp<R4>(m)); m = fmaxf(m, dpp<R8>(m)); m = mx32(mx16(m));
      if (lane < 2) ((LAS float*)(L + L_WM))[lane * 8 + wave] = m;
      BAR();
      {
        const f4 a = *(LAS f4*)(L + L_WM + (c & 1) * 32), b = *(LAS f4*)(L + L_WM + (c & 1) * 32 + 16);
        m = fmaxf(fmaxf(fmaxf(a.x, a.y), fmaxf(a.z, a.w)), fmaxf(fmaxf(b.x, b.y), fmaxf(b.z, b.w)));
      }
      const float pe = __builtin_amdgcn_exp2f((sc - m) * 1.4426950408889634f);
      v4u pa;
      pa.x = pk2(pe, dpp<0x102>(pe)); pa.y = dppu<0x104>(pa.x); pa.z = dppu<0x108>(pa.x); pa.w = dppu<0x10C>(pa.x);
      float l = pe + dpp<X2>(pe); l += dpp<R4>(l); l += dpp<R8>(l); l = sw16(l, l); l = sw32(l, l);
      if (lane < 2) ((LAS float*)(L + L_WL))[lane * 8 + wave] = l;
      STAMP(2);
      pair_rows(vv[0], vv[1]); pair_rows(vv[2], vv[3]); pair_rows(vv[4], vv[5]); pair_rows(vv[6], vv[7]);
      f4 o[8];
#pragma unroll
      for (int e = 0; e < 8; ++e) {
        const v4u bfr = {vv[0 + (e & 1)][e >> 1], vv[2 + (e & 1)][e >> 1], vv[4 + (e & 1)][e >> 1], vv[6 + (e & 1)][e >> 1]};
        o[e] = __builtin_amdgcn_mfma_f32_16x16x32_bf16(__builtin_bit_cast(bf8, pa), __builtin_bit_cast(bf8, bfr), f4{0, 0, 0, 0}, 0, 0, 0);
      }
      if (lane < 16) {
        LAS f4* ow = (LAS f4*)(L + L_OW + wave * 1024 + lane * 32);
        ow[0] = f4{o[0].x, o[1].x, o[2].x, o[3].x}; ow[1] = f4{o[4].x, o[5].x, o[6].x, o[7].x};
        ow[32] = f4{o[0].y, o[1].y, o[2].y, o[3].y}; ow[33] = f4{o[4].y, o[5].y, o[6].y, o[7].y};
      }
      STAMP(3);
      BAR();
      o_stage(rs, L, wv, kvh, wave, lane);
      STAMP(4);
      BAR();
    } else {
      tag = make_tag(ld8(rs, WS_EP));
      const v4u q = poll_tag(rs, WS_QG + (kvh * 64 + lane) * 16, tag);
      *(LAS v2u*)(L + L_QS + lane * 8) = v2u{q.x, q.z};
      if (lane == 0) *(LAS v2u*)(L + L_TAG) = tag;
      STAMP(1);
      BAR();
      BAR();
      float ml;
      { const float v = ((LAS float*)(L + L_WM))[lane & 15]; ml = max_lo3(v); }
      BAR();
      f4 s = {0.f, 0.f, 0.f, 0.f};
#pragma unroll
      for (int w = 0; w < 8; ++w) s += *(LAS f4*)(L + L_OW + w * 1024 + lane * 16);
      { const float l = sum_lo3(((LAS float*)(L + L_WL))[lane & 15]);
        if ((lane & 55) == 0) st8(rs, WS_PO + (kvh * 8 + sp) * PO_REC + 1024 + lane, v2u{nosent(ml), nosent(l)}); }
      st16(rs, WS_PO + (kvh * 8 + sp) * PO_REC + lane * 16, v4u{nosent(s.x), nosent(s.y), nosent(s.z), nosent(s.w)});
      STAMP(3);
      poll_tag(rs, WS_QG + lane * 256, tag);
      BAR();
      publish_ap(rs, L, kvh, sp, lane);
      STAMP(4);
    }
  } else if (j < 31) {
    const int kvh = 2 * xc + (j - 29);
    if (!helper) {
      v4u wv[4];
      load_wo(p, wv, kvh, 8, wave, lane);
      PIN();
      BAR();
      o_stage(rs, L, wv, kvh, wave, lane);
      BAR();
    } else {
      tag = make_tag(ld8(rs, WS_EP));
      poll_tag(rs, WS_QG + (kvh * 64 + lane) * 16, tag);
      BAR();
      BAR();
      publish_ap(rs, L, kvh, 8, lane);
      STAMP(4);
    }
    FLUSH();
    return;
  } else {
    return;
  }

  {
    const int col = 64 * wave + 4 * (lane & 15) + (lane >> 4), d0 = 3 * fi;
    const unsigned xb = ldh(p.x + col);
    v4u w1v, b1v, w2v; unsigned w1e, b1e, w2e;
    const int tw = threadIdx.x;
    if (!helper) {
      w1v = ldg(p.w1 + 8 * tw); b1v = ldg(p.b1 + 8 * tw); w2v = ldg(p.w2 + 8 * tw);
      w1e = ldh(p.w1 + 4096 + tw); b1e = ldh(p.b1 + 4096 + tw); w2e = ldh(p.w2 + 4096 + tw);
      PIN();
    }
    const unsigned b2b = ldh(p.b2);
    PIN();
    float ga, sb, av, gb;
    {
      const int off = WS_MG + (16 * wave + (lane & 15)) * 16;
      v4u g0, g1, g2, g3;
      poll_tag4(rs, off, 144 * 16, tag, g0, g1, g2, g3);
      const bool hi = lane & 32; const int sh = (lane & 16) ? 0 : 16;
      ga = __uint_as_float(((hi ? g0.z : g0.x) << sh) & 0xFFFF0000u);
      sb = __uint_as_float(((hi ? g1.z : g1.x) << sh) & 0xFFFF0000u);
      av = rb(1.f + __uint_as_float(((hi ? g2.z : g2.x) << sh) & 0xFFFF0000u));
      gb = __uint_as_float(((hi ? g3.z : g3.x) << sh) & 0xFFFF0000u);
    }
    STAMP(5);
    {
      const float v[8] = {av, av * av, av * sb, sb, sb * sb, 0.f, 0.f, 0.f};
      const float r = sum_lo3(red8_hi(v, lane));
      if ((lane & 7) == 0) ((LAS float*)(L + L_ST0))[wave * 8 + (lane >> 3)] = r;
      const unsigned k = (unsigned)(col - d0);
      if (k < 3u) { LAS float* own = (LAS float*)(L + L_OWN); own[4 + k] = av; own[8 + k] = sb; own[12 + k] = gb; }
    }
    f2 A0[5], A1[5], A2[5], A3[5], U0[5], U1[5];
    if (!helper) {
      asm volatile("" : "+v"(w1e), "+v"(b1e), "+v"(w2e));
      const float c1 = -2.3022081986f, c2 = -0.1029432397f;
#pragma unroll
      for (int i = 0; i < 5; ++i) {
        const unsigned a = i < 4 ? w1v[i] : w1e, b = i < 4 ? b1v[i] : b1e, c = i < 4 ? w2v[i] : w2e;
        const f2 wa = {bf2f(a), __uint_as_float(a & 0xFFFF0000u)}, ba = {bf2f(b), __uint_as_float(b & 0xFFFF0000u)}, wb = {bf2f(c), __uint_as_float(c & 0xFFFF0000u)};
        const f2 cw = c2 * wa * wa, cbb = c2 * ba * ba;
        A3[i] = cw * wa; A2[i] = 3.f * cw * ba; A1[i] = wa * (c1 + 3.f * cbb); A0[i] = ba * (c1 + cbb);
        U1[i] = wa * wb; U0[i] = ba * wb;
        asm volatile("" : "+v"(A0[i]), "+v"(A1[i]), "+v"(A2[i]), "+v"(A3[i]), "+v"(U0[i]), "+v"(U1[i]));
      }
    }
    BAR();
    float sA, sAA, sAS, sS, sSS;
    {
      float r = ((LAS float*)(L + L_ST0))[(lane & 7) * 8 + (lane >> 3)];
      r = sum_lo3(r) + ((LAS float*)(L + L_ST0))[64 + (lane >> 3)];
      sA = rdl(r, 0); sAA = rdl(r, 8); sAS = rdl(r, 16); sS = rdl(r, 24); sSS = rdl(r, 32);
    }
    const float av_d = ((LAS float*)(L + L_OWN))[4 + (lane & 3)], sb_d = ((LAS float*)(L + L_OWN))[8 + (lane & 3)];
    STAMP(6);
    float x1;
    {
      const int off = WS_AP + (wave * 16 + (lane >> 4)) * 256 + (lane & 15) * 16;
      v4u a0 = ld16(rs, off), a1 = ld16(rs, off + 1024), a2 = ld16(rs, off + 2048), a3 = ld16(rs, off + 3072);
      for (int s = 0; s < SPIN_CAP; ++s) {
        const bool k0 = __any(has_sent(a0)), k1 = __any(has_sent(a1)), k2 = __any(has_sent(a2)), k3 = __any(has_sent(a3));
        if (!(k0 | k1 | k2 | k3)) break;
        asm volatile("" ::: "memory");
        if (k0) a0 = ld16(rs, off);
        if (k1) a1 = ld16(rs, off + 1024);
        if (k2) a2 = ld16(rs, off + 2048);
        if (k3) a3 = ld16(rs, off + 3072);
      }
      STAMP(7);
      const f4 s = (__builtin_bit_cast(f4, a0) + __builtin_bit_cast(f4, a1)) + (__builtin_bit_cast(f4, a2) + __builtin_bit_cast(f4, a3));
      const float at = sw16(sw32(s.x, s.z), sw32(s.y, s.w));
      x1 = rb(bf2f(xb) + rb(ga * rb(at)));
      const float uu = x1 * av;
      const float v[8] = {x1, x1 * x1, uu, uu * uu, uu * av, uu * sb, 0.f, 0.f};
      const float r = sum_lo3(red8_hi(v, lane));
      if ((lane & 7) == 0) ((LAS float*)(L + L_ST1))[wave * 8 + (lane >> 3)] = r;
      const unsigned k = (unsigned)(col - d0);
      if (k < 3u) ((LAS float*)(L + L_OWN))[k] = x1;
    }
    BAR();
    const float x1_d = ((LAS float*)(L + L_OWN))[lane & 3];
    if (!helper) {
      float r = ((LAS float*)(L + L_ST1))[(lane & 7) * 8 + (lane >> 3)];
      r = sum_lo3(r) + ((LAS float*)(L + L_ST1))[64 + (lane >> 3)];
      const float sX = rdl(r, 0), sXX = rdl(r, 8), sU = rdl(r, 16), sUU = rdl(r, 24), sUA = rdl(r, 32), sUS = rdl(r, 40);
      const float mean = sX * (1.f / D), var = fmaxf(sXX * (1.f / D) - mean * mean, 0.f);
      const float m1 = rb(mean), r1 = __builtin_amdgcn_rcpf(rb(__builtin_amdgcn_sqrtf(rb(rb(var) + 1e-5f))));
      const float mu = (r1 * (sU - m1 * sA) + sS) * (1.f / D);
      const float e2 = (r1 * r1 * (sUU - 2.f * m1 * sUA + m1 * m1 * sAA) + 2.f * r1 * (sUS - m1 * sAS) + sSS) * (1.f / D);
      const float var2 = fmaxf(e2 - mu * mu, 0.f);
      const float m2 = rb(mu), r2 = __builtin_amdgcn_rcpf(rb(__builtin_amdgcn_sqrtf(rb(rb(var2) + 1e-5f))));
      const float z = rb(rb(x1_d - m1) * r1);
      const float h = rb(rb(z * av_d) + sb_d);
      const float t = rb(rb(h - m2) * r2);
      STAMP(8);
      const float t0 = rdl(t, 0), t1 = rdl(t, 1), t2 = rdl(t, 2);
      f2 acc[3];
#pragma unroll
      for (int d = 0; d < 3; ++d) {
        const float td = d == 0 ? t0 : d == 1 ? t1 : t2;
        const f2 tt = {td, td};
        acc[d] = f2{0.f, 0.f};
#pragma unroll
        for (int i = 0; i < 4; ++i) acc[d] = gelu2(A3[i], A2[i], A1[i], A0[i], U1[i], U0[i], tt, acc[d]);
      }
      const f2 e01 = gelu2(f2{A3[4].x, A3[4].x}, f2{A2[4].x, A2[4].x}, f2{A1[4].x, A1[4].x}, f2{A0[4].x, A0[4].x}, f2{U1[4].x, U1[4].x}, f2{U0[4].x, U0[4].x}, f2{t0, t1}, f2{acc[0].x + acc[0].y, acc[1].x + acc[1].y});
      const float a2 = fmaf(fmaf(fmaf(A3[4].x, t2, A2[4].x), t2, A1[4].x), t2, A0[4].x);
      const float e2s = fmaf(fmaf(U1[4].x, t2, U0[4].x), __builtin_amdgcn_rcpf(1.f + __builtin_amdgcn_exp2f(a2)), acc[2].x + acc[2].y);
      float q = sw16(sw32(e01.x, e2s), sw32(e01.y, 0.f));
      q += dpp<R8>(q); q = sum_lo3(q);
      if ((lane & 15) == 0) ((LAS float*)(L + L_FIN))[wave * 4 + (lane >> 4)] = q;
    }
    STAMP(9);
    BAR();
    if (helper) {
      if (lane < 3) {
        float y = 0.f;
#pragma unroll
        for (int w = 0; w < 8; ++w) y += ((LAS float*)(L + L_FIN))[w * 4 + lane];
        y = rb(rb(y) + bf2f(b2b));
        const float gbd = ((LAS float*)(L + L_OWN))[12 + lane];
        *(GAS u16*)(p.out + d0 + lane) = (u16)(__float_as_uint(rb(x1_d + rb(gbd * y))) >> 16);
      }
      if (fi == 0 && lane == 0) st8(rs, WS_EP, tag);
      STAMP(10);
    }
    FLUSH();
  }
}

extern "C" void kernel_launch(void* const* d_in, const int* in_sizes, int n_in, void* d_out, int out_size, void* d_ws, size_t ws_size, hipStream_t stream) {
  Params p;
  p.x = (const u16*)d_in[0]; p.kc = (const u16*)d_in[1]; p.vc = (const u16*)d_in[2]; p.wq = (const u16*)d_in[3]; p.wo = (const u16*)d_in[4];
  p.w1 = (const u16*)d_in[6]; p.b1 = (const u16*)d_in[7]; p.w2 = (const u16*)d_in[8]; p.b2 = (const u16*)d_in[9];
  p.c = (const u16*)d_in[10]; p.wm = (const u16*)d_in[11];
  p.out = (u16*)d_out; p.ws = (unsigned char*)d_ws;
  fused_k<<<NWG, NTHR, 0, stream>>>(p);
}
```

```cpp
#include <hip/hip_runtime.h>
#include <stdint.h>

typedef unsigned short u16;
typedef unsigned long long u64;
typedef unsigned v4u __attribute__((ext_vector_type(4)));
typedef unsigned v2u __attribute__((ext_vector_type(2)));
typedef float f4 __attribute__((ext_vector_type(4)));
typedef float f2 __attribute__((ext_vector_type(2)));
typedef __bf16 bf2 __attribute__((ext_vector_type(2)));
typedef __bf16 bf8 __attribute__((ext_vector_type(8)));
#define GAS __attribute__((address_space(1)))
#define LAS __attribute__((address_space(3)))

constexpr int D = 576, NMOD = 6 * D, SK = 8192, DH = 128, W = 2048, F = 4608, NQ = 4096;
constexpr int NWG = 256, NTHR = 320;
constexpr int PR = 16384, BL = 8192;
constexpr int WS_EP = 0;
constexpr int WS_XS = 1024;
constexpr int WS_NB = 6144;
constexpr int WS_XG = 1 * PR;
constexpr int WS_AP = 3 * PR;
constexpr int WS_ML = 8 * PR;
constexpr int WS_PO = 10 * PR;
constexpr int WS_QG = 26 * PR;
constexpr int WS_HG = 34 * PR;
constexpr int WS_MG = 66 * PR;
constexpr int WS_PB = 74 * PR;
constexpr int WS_END = WS_PB + 16 * PR;
constexpr int WQ_AT = 90;
constexpr int K_AT = 100;
constexpr int H_AT = 200;
constexpr int AQ_AT = 450;
constexpr int WO_AT = 450;
constexpr int M2_AT = 540;
constexpr int MG_AT = 690;
constexpr int GAP = 130;
constexpr int CODE_KB = 24;
constexpr int OOB = 0x70000000;
constexpr unsigned SENT = 0xFFFFFFFFu;
constexpr int SPIN_CAP = 1 << 17;
constexpr int AUX_SC1 = 16;

struct Params {
  const u16 *x, *kc, *vc, *wq, *wo, *w1, *b1, *w2, *b2, *c, *wm;
  u16* out;
  unsigned char* ws;
};

constexpr int L_K = 0;
constexpr int L_RED = 65536;
constexpr int L_HT = L_RED + 2048;
constexpr int L_QS = L_HT + 1024;
constexpr int L_WM = L_QS + 512;
constexpr int L_WL = L_WM + 32;
constexpr int L_OW = L_WL + 32;
constexpr int L_ST0 = L_OW + 4096;
constexpr int L_OWN = L_ST0 + 512;
constexpr int L_FIN = L_OWN + 64;
constexpr int L_TAG = L_FIN + 64;
constexpr int L_QP = L_TAG + 48;
constexpr int L_WO = L_QP + 3 * 4096;
constexpr int L_END = L_WO + 8 * 4608;
constexpr int L_BYTES = 124 * 1024;
static_assert(L_END <= L_BYTES, "lds");

__device__ __forceinline__ float bf2f(unsigned h) { return __uint_as_float(h << 16); }
__device__ __forceinline__ float rb(float x) { f2 v = {0.f, x}; return __builtin_bit_cast(float, __builtin_convertvector(v, bf2)); }
__device__ __forceinline__ unsigned pk2(float lo, float hi) { f2 v = {lo, hi}; return __builtin_bit_cast(unsigned, __builtin_convertvector(v, bf2)); }
__device__ __forceinline__ float dot2(unsigned a, unsigned b, float c) { return __builtin_amdgcn_fdot2_f32_bf16(__builtin_bit_cast(bf2, a), __builtin_bit_cast(bf2, b), c, false); }
template <int C> __device__ __forceinline__ unsigned dppu(unsigned x) { return (unsigned)__builtin_amdgcn_mov_dpp((int)x, C, 0xf, 0xf, true); }
template <int C> __device__ __forceinline__ float dpp(float x) { return __uint_as_float(dppu<C>(__float_as_uint(x))); }
constexpr int X1 = 0xB1, X2 = 0x4E, X7 = 0x141, R4 = 0x124, R8 = 0x128;
__device__ __forceinline__ float sw32(float a, float b) { auto r = __builtin_amdgcn_permlane32_swap(__float_as_uint(a), __float_as_uint(b), false, false); return __uint_as_float(r[0]) + __uint_as_float(r[1]); }
__device__ __forceinline__ float sw16(float a, float b) { auto r = __builtin_amdgcn_permlane16_swap(__float_as_uint(a), __float_as_uint(b), false, false); return __uint_as_float(r[0]) + __uint_as_float(r[1]); }
__device__ __forceinline__ float mx32(float a) { auto r = __builtin_amdgcn_permlane32_swap(__float_as_uint(a), __float_as_uint(a), false, false); return fmaxf(__uint_as_float(r[0]), __uint_as_float(r[1])); }
__device__ __forceinline__ float mx16(float a) { auto r = __builtin_amdgcn_permlane16_swap(__float_as_uint(a), __float_as_uint(a), false, false); return fmaxf(__uint_as_float(r[0]), __uint_as_float(r[1])); }
__device__ __forceinline__ float red8_hi(const float (&a)[8], int lane) {
  float v0 = sw32(a[0], a[4]), v1 = sw32(a[1], a[5]), v2 = sw32(a[2], a[6]), v3 = sw32(a[3], a[7]);
  float u0 = sw16(v0, v2), u1 = sw16(v1, v3);
  float t0 = u0 + dpp<R8>(u0), t1 = u1 + dpp<R8>(u1);
  return (lane & 8) ? t1 : t0;
}
__device__ __forceinline__ float sum_lo3(float v) { v += dpp<X1>(v); v += dpp<X2>(v); v += dpp<X7>(v); return v; }
__device__ __forceinline__ unsigned nosent(float v) { return min(__float_as_uint(v), SENT - 1u); }
__device__ __forceinline__ unsigned ldh(const u16* p) { return *(const GAS u16*)p; }
#define BAR() do { asm volatile("s_waitcnt lgkmcnt(0)" ::: "memory"); __builtin_amdgcn_s_barrier(); asm volatile("" ::: "memory"); __builtin_amdgcn_sched_barrier(0); } while (0)
#define PIN() __builtin_amdgcn_sched_barrier(0)
typedef __amdgpu_buffer_rsrc_t rsrc_t;
__device__ __forceinline__ rsrc_t mkrs(const void* p, int bytes) { return __builtin_amdgcn_make_buffer_rsrc((void*)p, 0, bytes, 0x00020000); }
__device__ __forceinline__ v4u ldb(rsrc_t r, int voff, int soff) { return __builtin_amdgcn_raw_buffer_load_b128(r, voff, soff, 2); }
__device__ __forceinline__ unsigned ldbh(rsrc_t r, int voff, int soff) { return __builtin_amdgcn_raw_buffer_load_b16(r, voff, soff, 0); }
__device__ __forceinline__ v4u ld16(rsrc_t r, int off) { return __builtin_amdgcn_raw_buffer_load_b128(r, off, 0, AUX_SC1); }
__device__ __forceinline__ v2u ld8(rsrc_t r, int off) { return __builtin_amdgcn_raw_buffer_load_b64(r, off, 0, AUX_SC1); }
__device__ __forceinline__ void st16(rsrc_t r, int off, v4u v) { __builtin_amdgcn_raw_buffer_store_b128(v, r, off, 0, AUX_SC1); }
__device__ __forceinline__ void st8(rsrc_t r, int off, v2u v) { __builtin_amdgcn_raw_buffer_store_b64(v, r, off, 0, AUX_SC1); }
__device__ __forceinline__ v2u make_tag(v2u e) {
  return v2u{__builtin_amdgcn_readfirstlane(e.x) + 0x9E3779B9u, __builtin_amdgcn_readfirstlane(e.y) + 0x7F4A7C15u};
}
__device__ __forceinline__ v4u poll_tag(rsrc_t r, int off, v2u tag) {
  v4u g;
  _Pragma("nounroll") for (int s = 0; s < SPIN_CAP; ++s) {
    asm volatile("" ::: "memory");
    g = ld16(r, off);
    if (__all(g.y == tag.x && g.w == tag.y)) break;
  }
  return g;
}

__device__ __forceinline__ float rdl(float v, int l) { return __uint_as_float(__builtin_amdgcn_readlane(__float_as_uint(v), l)); }
__device__ __forceinline__ unsigned umax4(v4u a) { return max(max(a.x, a.y), max(a.z, a.w)); }
__device__ __forceinline__ unsigned tag_dif(v4u g, v2u tag) { return (g.y ^ tag.x) | (g.w ^ tag.y); }
__device__ __forceinline__ void poll_tag4(rsrc_t r, int off, int d1, int d2, int d3, v2u tag, v4u& g0, v4u& g1, v4u& g2, v4u& g3) {
  _Pragma("nounroll") for (int s = 0; s < SPIN_CAP; ++s) {
    const unsigned e0 = tag_dif(g0, tag), e1 = tag_dif(g1, tag), e2 = tag_dif(g2, tag), e3 = tag_dif(g3, tag);
    if (__builtin_expect(!__any(((e0 | e1) | (e2 | e3)) != 0u), 1)) break;
    asm volatile("" ::: "memory");
    const bool some = !__all(e0 != 0u);
    if (!some) __builtin_amdgcn_s_sleep(3);
    if (__any(e0 != 0u)) g0 = ld16(r, off);
    if (some && __any(e1 != 0u)) g1 = ld16(r, off + d1);
    if (some && __any(e2 != 0u)) g2 = ld16(r, off + d2);
    if (some && __any(e3 != 0u)) g3 = ld16(r, off + d3);
  }
}
__device__ __forceinline__ void poll_all_q(rsrc_t r, int lane, v2u tag) {
  _Pragma("nounroll") for (int s = 0; s < SPIN_CAP; ++s) {
    asm volatile("" ::: "memory");
    const int o = WS_QG + (lane >> 4) * PR + (lane & 15) * 512;
    const v4u a = ld16(r, o), b = ld16(r, o + 4 * PR);
    if (!__any((tag_dif(a, tag) | tag_dif(b, tag)) != 0u)) break;
  }
}

__device__ __forceinline__ void row_fma(float (&acc)[8], v4u w, unsigned slo) {
  const unsigned shi = slo << 16;
#pragma unroll
  for (int k = 0; k < 4; ++k) { acc[2 * k] = dot2(w[k], slo, acc[2 * k]); acc[2 * k + 1] = dot2(w[k], shi, acc[2 * k + 1]); }
}
__device__ __forceinline__ void pair_rows(v4u& a, v4u& b) {
#pragma unroll
  for (int k = 0; k < 4; ++k) { const unsigned lo = __builtin_amdgcn_perm(b[k], a[k], 0x05040100u), hi = __builtin_amdgcn_perm(b[k], a[k], 0x07060302u); a[k] = lo; b[k] = hi; }
}
__device__ __forceinline__ void pair_fma(float (&acc)[8], v4u lo, v4u hi, unsigned s2) {
#pragma unroll
  for (int k = 0; k < 4; ++k) { acc[2 * k] = dot2(lo[k], s2, acc[2 * k]); acc[2 * k + 1] = dot2(hi[k], s2, acc[2 * k + 1]); }
}
__device__ __forceinline__ unsigned silu_bits(unsigned cb) {
  const float c = bf2f(cb);
  return __float_as_uint(rb(c * __builtin_amdgcn_rcpf(1.f + __builtin_amdgcn_exp2f(c * -1.4426950408889634f)))) >> 16;
}

__device__ __forceinline__ void o_poll(rsrc_t rs, int nb, int kvh, int wave, int lane, v4u& pa, v4u& pb, v2u& ml) {
  const int offo = WS_PO + kvh * PR + nb + (lane >> 3) * 1024 + wave * 256 + (lane & 7) * 16;
  const int offm = WS_ML + (kvh >> 3) * PR + nb + (kvh & 7) * 1024 + (lane >> 3) * 128 + wave * 16 + (wave >> 1) * 8;
  v4u a0 = ld16(rs, offo), a1 = ld16(rs, offo + 128), b0, b1; v2u a2 = ld8(rs, offm), b2;
  _Pragma("nounroll") for (int s = 0;; ++s) {
    __builtin_amdgcn_s_sleep(1);
    asm volatile("" ::: "memory");
    b0 = ld16(rs, offo); b1 = ld16(rs, offo + 128); b2 = ld8(rs, offm);
    PIN();
    if (!__any(max(max(umax4(a0), umax4(a1)), max(a2.x, a2.y)) == SENT) || s >= SPIN_CAP) { pa = a0; pb = a1; ml = a2; break; }
    __builtin_amdgcn_s_sleep(1);
    asm volatile("" ::: "memory");
    a0 = ld16(rs, offo); a1 = ld16(rs, offo + 128); a2 = ld8(rs, offm);
    PIN();
    if (!__any(max(max(umax4(b0), umax4(b1)), max(b2.x, b2.y)) == SENT)) { pa = b0; pb = b1; ml = b2; break; }
  }
}
__device__ __forceinline__ void o_stage(LAS unsigned char* L, const v4u (&wv)[8], int wave, int lane, v4u pa, v4u pb, v2u ml) {
  const float m = __uint_as_float(ml.x);
  const float M = mx32(mx16(fmaxf(m, dpp<R8>(m))));
  const float e = __builtin_amdgcn_exp2f((m - M) * 1.4426950408889634f);
  float Ls = e * __uint_as_float(ml.y); Ls += dpp<R8>(Ls); Ls = sw16(Ls, Ls); Ls = sw32(Ls, Ls);
  const f4 a = e * __builtin_bit_cast(f4, pa), b = e * __builtin_bit_cast(f4, pb);
  float o0 = sw16(sw32(a.x, b.x), sw32(a.z, b.z)), o1 = sw16(sw32(a.y, b.y), sw32(a.w, b.w));
  const float ri = __builtin_amdgcn_rcpf(Ls);
  const int pk = (int)pk2((o0 + dpp<R8>(o0)) * ri, (o1 + dpp<R8>(o1)) * ri);
  const int sl = (lane >> 3) * 4;
  const unsigned q0 = (unsigned)__builtin_amdgcn_ds_bpermute(sl, pk), q1 = (unsigned)__builtin_amdgcn_ds_bpermute(sl + 64, pk);
  const unsigned q2 = (unsigned)__builtin_amdgcn_ds_bpermute(sl + 128, pk), q3 = (unsigned)__builtin_amdgcn_ds_bpermute(sl + 192, pk);
  const bf8 av = __builtin_bit_cast(bf8, v4u{q0, q1, q2, q3});
  f4 d[8];
#pragma unroll
  for (int e = 0; e < 8; ++e) d[e] = __builtin_amdgcn_mfma_f32_16x16x32_bf16(av, __builtin_bit_cast(bf8, wv[e]), f4{0, 0, 0, 0}, 0, 0, 0);
  if ((lane >> 3) == 0 || (lane >> 3) == 5) {
    LAS f4* r = (LAS f4*)(L + L_RED + (wave * 2 + (lane >> 5)) * 256 + (lane & 7) * 32);
    r[0] = f4{d[0].x, d[1].x, d[2].x, d[3].x}; r[1] = f4{d[4].x, d[5].x, d[6].x, d[7].x};
  }
}
__device__ __forceinline__ void load_wo(const Params& p, v4u (&wv)[8], int kvh, int cb, int wave, int lane) {
  const rsrc_t r = mkrs(p.wo, NQ * D * 2);
  const int vo = (lane >> 3) * (4 * D * 2) + (lane & 7) * 16, so = ((2 * kvh * DH + 64 * wave) * D + 64 * cb) * 2;
#pragma unroll
  for (int i = 0; i < 8; ++i) wv[i] = ldb(r, vo + (i & 3) * D * 2, so + (i >> 2) * 32 * D * 2);
}
__device__ __forceinline__ void frag8(v4u (&w)[8]) {
  v4u f[8];
#pragma unroll
  for (int e = 0; e < 8; ++e)
#pragma unroll
    for (int k = 0; k < 4; ++k) f[e][k] = __builtin_amdgcn_perm(w[2 * k + 1][e >> 1], w[2 * k][e >> 1], (e & 1) ? 0x07060302u : 0x05040100u);
#pragma unroll
  for (int e = 0; e < 8; ++e) w[e] = f[e];
  asm volatile("" : "+v"(w[0]), "+v"(w[1]), "+v"(w[2]), "+v"(w[3]), "+v"(w[4]), "+v"(w[5]), "+v"(w[6]), "+v"(w[7]));
}
__device__ __forceinline__ void publish_ap(rsrc_t rs, LAS unsigned char* L, int kvh, int cb, int lane) {
  const LAS f4* r = (const LAS f4*)(L + L_RED) + (lane & 15);
  const f4 v = ((r[0] + r[16]) + (r[32] + r[48])) + ((r[64] + r[80]) + (r[96] + r[112]));
  st16(rs, lane < 32 ? WS_AP + (cb >> 1) * PR + (lane >> 4) * BL + (cb & 1) * 4096 + kvh * 256 + (lane & 15) * 16 : OOB, v4u{nosent(v.x), nosent(v.y), nosent(v.z), nosent(v.w)});
}
__device__ __forceinline__ float pick_score(f4 a, f4 b, int c) {
  const bool t = c & 8, hi = c & 4;
  const float x = t ? b.x : a.x, y = t ? b.y : a.y, z = t ? b.z : a.z, w = t ? b.w : a.w;
  const float e0 = hi ? z : x, e1 = hi ? w : y;
  return rb(rb((c & 2) ? e1 : e0) * 0.08838834764831845f);
}
__device__ __forceinline__ f2 unpk(unsigned a) { return f2{bf2f(a), __uint_as_float(a & 0xFFFF0000u)}; }
__device__ __forceinline__ void x_sums(LAS unsigned char* L, unsigned xb, int wave, int lane) {
  const f2 xv = unpk(xb);
  float v = sw32(xv.x + xv.y, xv.x * xv.x + xv.y * xv.y); v += dpp<R8>(v); v = sw16(v, v); v = sum_lo3(v);
  if ((lane & 31) == 0) ((LAS float*)(L + L_ST0))[wave * 2 + (lane >> 5)] = v;
}
template <class More> __device__ __forceinline__ int probe_gap(rsrc_t rs, int o1, int o2, More more) {
  v4u a = ld16(rs, o1), b = ld16(rs, o2);
  PIN();
  more();
  PIN();
  asm volatile("" : "+v"(a));
  const u64 t1 = __builtin_amdgcn_s_memtime();
  PIN();
  asm volatile("s_waitcnt lgkmcnt(0)" : "+v"(b) :: "memory");
  const u64 t2 = __builtin_amdgcn_s_memtime();
  PIN();
  return (int)(t2 - t1);
}
struct Mlp { f2 A0[9], A1[9], A2[9], A3[9], U0[9], U1[9]; };
struct MlpRaw { v4u w1[2], b1[2], w2[2]; unsigned e1, eb, e2; };
__device__ __forceinline__ void mlp_load(const Params& p, MlpRaw& r, int t) {
  const rsrc_t r1 = mkrs(p.w1, F * 2), rb_ = mkrs(p.b1, F * 2), r2 = mkrs(p.w2, F * 2);
#pragma unroll
  for (int i = 0; i < 2; ++i) {
    r.w1[i] = __builtin_amdgcn_raw_buffer_load_b128(r1, t * 16, i * 4096, 0);
    r.b1[i] = __builtin_amdgcn_raw_buffer_load_b128(rb_, t * 16, i * 4096, 0);
    r.w2[i] = __builtin_amdgcn_raw_buffer_load_b128(r2, t * 16, i * 4096, 0);
  }
  r.e1 = __builtin_amdgcn_raw_buffer_load_b32(r1, t * 4, 8192, 0); r.eb = __builtin_amdgcn_raw_buffer_load_b32(rb_, t * 4, 8192, 0); r.e2 = __builtin_amdgcn_raw_buffer_load_b32(r2, t * 4, 8192, 0);
}
__device__ __forceinline__ void mlp_prep(const MlpRaw& r, Mlp& m) {
  const float c1 = -2.3022081986f, c2 = -0.1029432397f;
#pragma unroll
  for (int i = 0; i < 9; ++i) {
    const f2 wa = unpk(i < 8 ? r.w1[i >> 2][i & 3] : r.e1), ba = unpk(i < 8 ? r.b1[i >> 2][i & 3] : r.eb), wb = unpk(i < 8 ? r.w2[i >> 2][i & 3] : r.e2);
    const f2 cw = c2 * wa * wa, cb = c2 * ba * ba;
    m.A3[i] = cw * wa; m.A2[i] = 3.f * cw * ba; m.A1[i] = wa * (c1 + 3.f * cb); m.A0[i] = ba * (c1 + cb);
    m.U1[i] = wa * wb; m.U0[i] = ba * wb;
    asm volatile("" : "+v"(m.A0[i]), "+v"(m.A1[i]), "+v"(m.A2[i]), "+v"(m.A3[i]), "+v"(m.U0[i]), "+v"(m.U1[i]));
  }
}

#define KDMA4(I) asm volatile("s_mov_b32 %0, m0\n\ts_mov_b32 m0, %5\n\ts_nop 4\n\tbuffer_load_dwordx4 %1, %6, %7 offen nt lds\n\tbuffer_load_dwordx4 %2, %6, %7 offen offset:1024 nt lds\n\t" \
                     "buffer_load_dwordx4 %3, %6, %7 offen offset:2048 nt lds\n\tbuffer_load_dwordx4 %4, %6, %7 offen offset:3072 nt lds\n\ts_mov_b32 m0, %0" \
                     : "=&s"(keep) : "v"(kvo), "v"(kvo ^ 64), "v"(kvo ^ 128), "v"(kvo ^ 192), "s"(kl0 + (I) * 4096), "s"(rk), "s"(kso + (((I) + rot) & 3) * 4096) : "memory")

__global__ void __launch_bounds__(NTHR, 2) fused_k(Params p) {
  __shared__ __attribute__((aligned(16))) unsigned char smem[L_BYTES];
  LAS unsigned char* L = (LAS unsigned char*)smem;
  unsigned keep;
  {
    u64 pc0;
    asm volatile("s_getpc_b64 %0" : "=s"(pc0));
    const v4u rcode = {(unsigned)pc0 & ~127u, (unsigned)(pc0 >> 32) & 0xFFFFu, (unsigned)(CODE_KB * 1024), 0x00020000u};
    asm volatile("s_mov_b32 %0, m0\n\ts_mov_b32 m0, %3\n\ts_nop 0\n\tbuffer_load_dwordx4 %1, %2, 0 offen lds\n\ts_mov_b32 m0, %0"
                 : "=&s"(keep) : "v"((blockIdx.x >> 3) * 1024 + (threadIdx.x & 63) * 16 + (threadIdx.x >> 6) * 32768), "s"(rcode), "i"(L_QP + 8192) : "memory");
  }
  const int lane = threadIdx.x & 63, wave = __builtin_amdgcn_readfirstlane(threadIdx.x >> 6), t = threadIdx.x;
  const int xc = blockIdx.x & 7, j = blockIdx.x >> 3;
  const bool helper = wave == 4;
  int jj = j;
  const u64 tstart = __builtin_amdgcn_s_memrealtime();
  asm volatile("" :: "s"(p.x), "s"(p.kc), "s"(p.vc), "s"(p.wq), "s"(p.wo), "s"(p.w1), "s"(p.b1), "s"(p.w2), "s"(p.b2), "s"(p.c), "s"(p.wm), "s"(p.out), "s"(p.ws));
  const rsrc_t rs = mkrs(p.ws, WS_END);
  int nb = 0;
  int fi;
  v2u tag;
  Mlp mw;
  const int g8 = lane >> 3, ch = lane & 7;
  v4u wv[9];
  unsigned xb = 0, xo = 0, b2b = 0;
  const int hcol = 64 * wave + 2 * (lane & 31) + (lane >> 5);
  v2u ep0;
  if (j >= 16 && helper) {
    const int o = WS_PB + ((blockIdx.x - 128) >> 3) * PR + (lane >> 3) * 1024 + xc * 128 + (lane & 7) * 16;
    asm volatile("s_waitcnt vmcnt(0)" ::: "memory");
    const int g01 = probe_gap(rs, o ^ 128, (o ^ 128) + BL, [&] { ep0 = ld8(rs, WS_EP); b2b = ldh(p.b2); });
    tag = make_tag(ep0);
    asm volatile("" : "+v"(b2b));
    const int hint_at = j == 16 && lane < 8 ? WS_NB + xc * 128 + lane * 16 : OOB;
    const int g10 = probe_gap(rs, o + BL, o, [&] { st16(rs, hint_at, v4u{g01 > GAP ? 0u : (unsigned)BL, tag.x, 0u, tag.y}); });
    nb = g10 > GAP ? BL : 0;
    asm volatile("s_waitcnt vmcnt(0)" ::: "memory");
  }
  if (__builtin_expect(j < 16, 1)) {
    const bool has_m = j < 10 && j != 3;
    const int mb = has_m ? j - (j > 3) : 0;
    if (!helper) {
      const int r0 = 144 * (xc >> 1) + 36 * wave, so = (r0 * NMOD + 64 * (9 * (xc & 1) + mb)) * 2;
      const int non = has_m ? 0 : OOB, tail = g8 < 4 ? non : OOB;
      const int vo = g8 * (NMOD * 2) + ch * 16;
      const rsrc_t rwm = mkrs(p.wm, D * NMOD * 2), rc = mkrs(p.c, D * 2);
      v4u mv[5]; unsigned cv[5];
#pragma unroll
      for (int i = 0; i < 5; ++i) cv[i] = ldbh(rc, g8 * 2 + i * 16 + (i < 4 ? non : tail), r0 * 2);
#pragma unroll
      for (int i = 0; i < 5; ++i) mv[i] = ldb(rwm, vo + (i < 4 ? non : tail), so + i * 8 * NMOD * 2);
      xb = __builtin_amdgcn_raw_buffer_load_b32(mkrs(p.x, D * 2), t * 4, 0, 0); xo = ldbh(mkrs(p.x, D * 2), hcol * 2 + 576 * (xc & 1), 0);
      PIN();
      __builtin_amdgcn_s_barrier();
      while (has_m && __builtin_amdgcn_s_memrealtime() - tstart < WQ_AT) { }
      PIN();
      const rsrc_t r = mkrs(p.wq, D * NQ * 2);
      const int vq = g8 * (NQ * 2) + ch * 16, sq = ((288 * (xc & 1) + 72 * wave) * NQ + 64 * (16 * (xc >> 1) + j)) * 2;
#pragma unroll
      for (int i = 0; i < 9; ++i) wv[i] = ldb(r, vq, sq + i * 8 * NQ * 2);
      PIN();
      float acc[8] = {0, 0, 0, 0, 0, 0, 0, 0};
#pragma unroll
      for (int i = 0; i < 5; ++i) { cv[i] = silu_bits(cv[i]); asm volatile("" : "+v"(cv[i])); }
      PIN();
#pragma unroll
      for (int i = 0; i < 5; ++i) row_fma(acc, mv[i], cv[i]);
      ((LAS float*)(L + L_RED))[wave * 64 + 8 * ch + g8] = red8_hi(acc, lane);
      x_sums(L, xb, wave, lane);
      asm volatile("" : "+v"(xo));
      BAR();
    } else {
      const v2u ep = ld8(rs, WS_EP);
      xb = __builtin_amdgcn_raw_buffer_load_b32(mkrs(p.x, D * 2), t * 4, 0, 0); xo = ldbh(mkrs(p.x, D * 2), hcol * 2 + 576 * (xc & 1), 0);
      b2b = ldh(p.b2);
      __builtin_amdgcn_s_barrier();
      __builtin_amdgcn_s_setprio(2);
      tag = make_tag(ep);
      if (lane == 0) *(LAS v4u*)(L + L_TAG) = v4u{tag.x, tag.y, (unsigned)nb, 0u};
      x_sums(L, xb, wave, lane);
      asm volatile("" : "+v"(xo), "+v"(b2b));
      BAR();
      const LAS f2* r = (const LAS f2*)(L + L_RED) + (lane & 31);
      const f2 sm = (r[0] + r[32]) + (r[64] + r[96]);
      const int ho = has_m ? WS_HG + ((xc & 1) * 4 + (xc >> 1)) * PR + (lane >> 5) * BL + (mb * 32 + (lane & 31)) * 16 : OOB;
#pragma unroll
      for (int k = 0; k < 4; ++k) st16(rs, ho + k * 8 * PR, v4u{__float_as_uint(sm.x), tag.x, __float_as_uint(sm.y), tag.y});
    }
  }
  asm volatile("" : "+s"(jj));
  if (jj < 16) {
    const int qb = 16 * (xc >> 1) + j, half = xc & 1, kvh = qb >> 2;
    const int g = g8;
    const rsrc_t rx = mkrs(p.x, D * 2);
    { const v4u tg = *(LAS v4u*)(L + L_TAG); tag.x = __builtin_amdgcn_readfirstlane(tg.x); tag.y = __builtin_amdgcn_readfirstlane(tg.y); nb = __builtin_amdgcn_readfirstlane(tg.z); }
    if (helper) {
      const v4u sv = {SENT, SENT, SENT, SENT};
      const int part = (qb & 3) * 2 + half, ln8 = part + 8 * ((lane >> 3) & 3), cp = (lane >> 5) * BL;
      st16(rs, WS_PO + kvh * PR + part * 1024 + lane * 16, sv);
      st16(rs, WS_PO + kvh * PR + BL + part * 1024 + lane * 16, sv);
      st16(rs, (lane & 31) >= 24 ? WS_ML + (kvh >> 3) * PR + cp + (kvh & 7) * 1024 + part * 128 + (lane & 7) * 16
               : ln8 < 18 ? WS_AP + (ln8 >> 2) * PR + cp + ((ln8 >> 1) & 1) * 4096 + kvh * 256 + (ln8 & 1) * 128 + (lane & 7) * 16 : OOB, sv);
    }
    float ln;
    {
      const f4 a = *(LAS f4*)(L + L_ST0), b = *(LAS f4*)(L + L_ST0 + 16); const f2 c = *(LAS f2*)(L + L_ST0 + 32);
      const float mean = ((a.x + a.z) + (b.x + b.z) + c.x) * (1.f / D), var = fmaxf(((a.y + a.w) + (b.y + b.w) + c.y) * (1.f / D) - mean * mean, 0.f);
      const float sq = rb(__builtin_amdgcn_sqrtf(rb(rb(var) + 1e-5f)));
      ln = rb(rb(bf2f(xo) - rb(mean)) * __builtin_amdgcn_rcpf(sq));
    }
    asm volatile("" : "+v"(ln));
    {
      const int pr = (32 * wave + (lane & 31)) % 144;
      int off = WS_HG + (xc >> 1) * 8 * PR + (144 * half + pr) * 16 + (lane >> 5) * 2 * PR;
      while (__builtin_amdgcn_s_memrealtime() - tstart < H_AT - 30) __builtin_amdgcn_s_sleep(1);
      {
        v4u hint;
        asm volatile("s_load_dwordx4 %0, %1, 0x0\n\ts_waitcnt lgkmcnt(0)" : "=s"(hint) : "s"(p.ws + WS_NB + xc * 128) : "memory");
        nb = (hint.y == tag.x && hint.w == tag.y) ? (int)(hint.x & BL) : (int)(((__builtin_amdgcn_s_getreg(20 | (3 << 11)) >> 2) ^ (unsigned)((u64)p.ws >> 21) ^ (unsigned)((u64)p.ws >> 13) ^ 1u) & 1u) * BL;
      }
      const int hw = hcol / 72, rem = hcol - 72 * hw, hoff = L_HT + (hw * 8 + (rem & 7)) * 32 + (rem >> 3) * 2;
      v4u g0, g1, g2, g3;
      while (__builtin_amdgcn_s_memrealtime() - tstart < H_AT) __builtin_amdgcn_s_sleep(1);
      off += nb;
      g0 = ld16(rs, off); g1 = ld16(rs, off + PR); g2 = ld16(rs, off + 4 * PR); g3 = ld16(rs, off + 5 * PR);
      PIN();
      if (!helper) {
        pair_rows(wv[0], wv[1]); pair_rows(wv[2], wv[3]); pair_rows(wv[4], wv[5]); pair_rows(wv[6], wv[7]);
        asm volatile("" : "+v"(wv[0]), "+v"(wv[1]), "+v"(wv[2]), "+v"(wv[3]), "+v"(wv[4]), "+v"(wv[5]), "+v"(wv[6]), "+v"(wv[7]), "+v"(wv[8]));
      }
      poll_tag4(rs, off, PR, 4 * PR, 5 * PR, tag, g0, g1, g2, g3);
      const float sa = rb(sw32(__uint_as_float(g0.x) + __uint_as_float(g1.x), __uint_as_float(g0.z) + __uint_as_float(g1.z)));
      const float ca = rb(sw32(__uint_as_float(g2.x) + __uint_as_float(g3.x), __uint_as_float(g2.z) + __uint_as_float(g3.z)));
      const float h = rb(rb(ln * rb(1.f + ca)) + sa);
      if (hcol < 288) *(LAS u16*)(L + hoff) = (u16)(__float_as_uint(h) >> 16);
    }
    BAR();
    if (!helper) {
      const v4u hp = *(LAS v4u*)(L + L_HT + (wave * 8 + g) * 32);
      const unsigned h8 = *(LAS u16*)(L + L_HT + (wave * 8 + g) * 32 + 16);
      float acc[8] = {0, 0, 0, 0, 0, 0, 0, 0};
      pair_fma(acc, wv[0], wv[1], hp.x); pair_fma(acc, wv[2], wv[3], hp.y); pair_fma(acc, wv[4], wv[5], hp.z); pair_fma(acc, wv[6], wv[7], hp.w);
      row_fma(acc, wv[8], h8);
      ((LAS float*)(L + L_RED))[wave * 64 + 8 * ch + g] = red8_hi(acc, lane);
    } else {
      asm volatile("s_waitcnt vmcnt(0)" ::: "memory");
    }
    BAR();
    if (helper) {
      const LAS f2* r = (const LAS f2*)(L + L_RED) + (lane & 31);
      const f2 s = (r[0] + r[32]) + (r[64] + r[96]);
      st16(rs, WS_QG + (half * 4 + (qb >> 4)) * PR + (lane >> 5) * BL + ((qb & 15) * 32 + (lane & 31)) * 16, v4u{__float_as_uint(s.x), tag.x, __float_as_uint(s.y), tag.y});
    }
    BAR();
    nb = __builtin_amdgcn_readfirstlane(poll_tag(rs, WS_NB + xc * 128, tag).x) & BL;
    if (j >= 14) {
      const int kv = 2 * xc + (j - 14);
      if (!helper) {
        v4u wo8[8];
        load_wo(p, wo8, kv, 8, wave, lane);
        frag8(wo8);
        BAR();
        { v4u pa, pb; v2u ml; o_poll(rs, nb, kv, wave, lane, pa, pb, ml); o_stage(L, wo8, wave, lane, pa, pb, ml); }
        BAR();
      } else {
        while (__builtin_amdgcn_s_memrealtime() - tstart < AQ_AT) __builtin_amdgcn_s_sleep(4);
        poll_all_q(rs, lane, tag);
        BAR();
        BAR();
        publish_ap(rs, L, kv, 8, lane);
      }
      return;
    } else if (j >= 8 && j != 9) {
      {
        if ((j == 8 || j == 10 || (j == 13 && (xc & 3) == 0)) && helper) {
          const int blk = j == 8 ? xc : j == 10 ? xc ^ 4 : 8, col = 64 * blk + 4 * (lane & 15) + (lane >> 4);
          const unsigned xb1 = ldbh(rx, col * 2, 0);
          while (__builtin_amdgcn_s_memrealtime() - tstart < AQ_AT) __builtin_amdgcn_s_sleep(4);
          poll_all_q(rs, lane, tag);
          v4u g0, g1, g2, g3, h0, h1, h2, h3;
          const int mo = WS_MG + nb + (col >> 1) * 16;
          while (__builtin_amdgcn_s_memrealtime() - tstart < MG_AT) __builtin_amdgcn_s_sleep(2);
          _Pragma("nounroll") for (int s = 0; s < SPIN_CAP; ++s) {
            asm volatile("" ::: "memory");
            g0 = ld16(rs, mo); g1 = ld16(rs, mo + 1 * PR); g2 = ld16(rs, mo + 2 * PR); g3 = ld16(rs, mo + 3 * PR);
            h0 = ld16(rs, mo + 4 * PR); h1 = ld16(rs, mo + 5 * PR); h2 = ld16(rs, mo + 6 * PR); h3 = ld16(rs, mo + 7 * PR);
            if (!__any((tag_dif(g0, tag) | tag_dif(g1, tag) | tag_dif(g2, tag) | tag_dif(g3, tag) | tag_dif(h0, tag) | tag_dif(h1, tag) | tag_dif(h2, tag) | tag_dif(h3, tag)) != 0u)) break;
          }
          const bool od = col & 1;
#define MSUM(A, B) rb(__uint_as_float(od ? A.z : A.x) + __uint_as_float(od ? B.z : B.x))
          const float ga = MSUM(g0, h0), sb = MSUM(g1, h1), av = rb(1.f + MSUM(g2, h2));
          const unsigned w1 = (__float_as_uint(sb) >> 16) | __float_as_uint(MSUM(g3, h3));
          float r0;
          { const float v[8] = {av, av * av, av * sb, sb, sb * sb, 0.f, 0.f, 0.f}; r0 = sum_lo3(red8_hi(v, lane)); }
          asm volatile("" : "+v"(r0));
          const int off = WS_AP + (blk >> 1) * PR + nb + (blk & 1) * 4096 + (lane >> 4) * 256 + (lane & 15) * 16;
          v4u a0, a1, a2, a3;
          {
#define AP4(P) do { P##0 = ld16(rs, off); P##1 = ld16(rs, off + 1024); P##2 = ld16(rs, off + 2048); P##3 = ld16(rs, off + 3072); PIN(); } while (0)
#define APOK(P) !__any(max(max(umax4(P##0), umax4(P##1)), max(umax4(P##2), umax4(P##3))) == SENT)
            v4u c0, c1, c2, c3, d0, d1, d2, d3;
            AP4(c);
            _Pragma("nounroll") for (int s = 0;; ++s) {
              __builtin_amdgcn_s_sleep(1);
              asm volatile("" ::: "memory");
              AP4(d);
              if (APOK(c) || s >= SPIN_CAP) { a0 = c0; a1 = c1; a2 = c2; a3 = c3; break; }
              __builtin_amdgcn_s_sleep(1);
              asm volatile("" ::: "memory");
              AP4(c);
              if (APOK(d)) { a0 = d0; a1 = d1; a2 = d2; a3 = d3; break; }
            }
          }
          const f4 sm = (__builtin_bit_cast(f4, a0) + __builtin_bit_cast(f4, a1)) + (__builtin_bit_cast(f4, a2) + __builtin_bit_cast(f4, a3));
          const float at = sw16(sw32(sm.x, sm.z), sw32(sm.y, sm.w));
          const float x1 = rb(bf2f(xb1) + rb(ga * rb(at)));
          { const v4u xv = {(__float_as_uint(x1) >> 16) | __float_as_uint(av), tag.x, w1, tag.y}; const int xo_ = WS_XG + (blk >> 3) * PR + (blk & 7) * 1024 + (col & 63) * 16; st16(rs, xo_, xv); st16(rs, xo_ + BL, xv); }
          const float uu = x1 * av;
          const float v[8] = {x1, x1 * x1, uu, uu * uu, uu * av, uu * sb, 0.f, 0.f};
          const float r1 = sum_lo3(red8_hi(v, lane));
          st16(rs, WS_XS + (lane & 1) * BL + (((lane >> 1) & 3) * 9 + blk) * 128 + (lane >> 3) * 16, v4u{__float_as_uint(r1), tag.x, __float_as_uint(r0), tag.y});
        }
        return;
      }
    }
    {
      const int u = 9 * xc + (j == 9 ? 8 : j), mh = u >= 36, mblk = u - 36 * mh;
      if (!helper) {
        const rsrc_t rwm = mkrs(p.wm, D * NMOD * 2), rc = mkrs(p.c, D * 2);
        unsigned c2[9]; v4u w2[9];
#pragma unroll
        for (int i = 0; i < 9; ++i) c2[i] = ldbh(rc, g * 2 + i * 16, (288 * mh + 72 * wave) * 2);
        while (__builtin_amdgcn_s_memrealtime() - tstart < M2_AT) __builtin_amdgcn_s_sleep(2);
#pragma unroll
        for (int i = 0; i < 9; ++i) w2[i] = ldb(rwm, g * (NMOD * 2) + ch * 16, ((288 * mh + 72 * wave + 8 * i) * NMOD + 64 * (18 + mblk)) * 2);
        PIN();
#pragma unroll
        for (int i = 0; i < 9; ++i) { c2[i] = silu_bits(c2[i]); asm volatile("" : "+v"(c2[i])); }
        PIN();
        float acc[8] = {0, 0, 0, 0, 0, 0, 0, 0};
#pragma unroll
        for (int i = 0; i < 9; ++i) row_fma(acc, w2[i], c2[i]);
        ((LAS float*)(L + L_RED))[wave * 64 + 8 * ch + g] = red8_hi(acc, lane);
      }
      BAR();
      if (helper) {
        const LAS f2* r = (const LAS f2*)(L + L_RED) + (lane & 31);
        const f2 sm = (r[0] + r[32]) + (r[64] + r[96]);
        st16(rs, WS_MG + (mh * 4 + mblk / 9) * PR + (lane >> 5) * BL + ((mblk % 9) * 32 + (lane & 31)) * 16, v4u{__float_as_uint(sm.x), tag.x, __float_as_uint(sm.y), tag.y});
      }
      if (j == 9) return;
    }
    fi = 128 + 8 * xc + j;
    if (!helper) { MlpRaw raw; mlp_load(p, raw, t); mlp_prep(raw, mw); }
  } else {
    const int kvh = 2 * xc + ((j - 16) >> 3), sp = j & 7;
    fi = kvh * 8 + sp;
    if (!helper) {
      const int g = lane >> 4, c = lane & 15;
      const int kb = (kvh * SK + (SK - W) + 256 * sp + 64 * wave) * DH;
      LAS unsigned char* kt = L + L_K + wave * 16384;
      const int rot = sp + wave + kvh;
      MlpRaw raw;
      v4u vv[2][8], wo8[8];
      while (__builtin_amdgcn_s_memrealtime() - tstart < K_AT) __builtin_amdgcn_s_sleep(1);
      {
        const int kvo = g * 256 + ((c ^ g) << 4), kl0 = L_K + wave * 16384, kso = kb * 2;
        const v4u rk = {(unsigned)(u64)p.kc, (unsigned)((u64)p.kc >> 32) & 0xFFFFu, (unsigned)(16 * SK * DH * 2), 0x00020000u};
        KDMA4(0); KDMA4(1); KDMA4(2); KDMA4(3);
      }
      const v4u rv = {(unsigned)(u64)p.vc, (unsigned)((u64)p.vc >> 32) & 0xFFFFu, (unsigned)(16 * SK * DH * 2), 0x00020000u};
#define VASM4(H) asm volatile("buffer_load_dwordx4 %0, %4, %5, %6 offen nt\n\tbuffer_load_dwordx4 %1, %4, %5, %6 offen offset:256 nt\n\t" \
                     "buffer_load_dwordx4 %2, %4, %5, %6 offen offset:512 nt\n\tbuffer_load_dwordx4 %3, %4, %5, %6 offen offset:768 nt" \
                     : "=&v"(vv[(H) >> 1][4 * ((H) & 1)]), "=&v"(vv[(H) >> 1][4 * ((H) & 1) + 1]), "=&v"(vv[(H) >> 1][4 * ((H) & 1) + 2]), "=&v"(vv[(H) >> 1][4 * ((H) & 1) + 3]) \
                     : "v"(g * 1024 + c * 16), "s"(rv), "s"(kb * 2 + (((H) + rot) & 3) * 4096) : "memory")
      VASM4(0); VASM4(1); VASM4(2); VASM4(3);
      asm volatile("s_waitcnt vmcnt(16)" ::: "memory");
      v4u kf[16];
#pragma unroll
      for (int T = 0; T < 4; ++T)
#pragma unroll
        for (int s = 0; s < 4; ++s) kf[T * 4 + s] = *(LAS v4u*)(kt + (16 * T + c) * 256 + ((4 * s + g) ^ c) * 16);
#define VWAIT(G, N) asm volatile("s_waitcnt vmcnt(%8)" : "+v"(vv[G][0]), "+v"(vv[G][1]), "+v"(vv[G][2]), "+v"(vv[G][3]), "+v"(vv[G][4]), "+v"(vv[G][5]), "+v"(vv[G][6]), "+v"(vv[G][7]) : "i"(N) : "memory")
      PIN();
      VWAIT(0, 8);
      frag8(vv[0]);
      asm volatile("s_waitcnt vmcnt(4)" ::: "memory");
      __builtin_amdgcn_s_barrier();
      __builtin_amdgcn_s_sleep(1);
      PIN();
      VWAIT(1, 0);
      frag8(vv[1]);
#define WO_DMA() do { \
        const v4u ro = {(unsigned)(u64)p.wo, (unsigned)((u64)p.wo >> 32) & 0xFFFFu, (unsigned)(NQ * D * 2), 0x00020000u}; \
        const int vo = (lane >> 3) * (4 * D * 2) + (lane & 7) * 16, so = ((2 * kvh * DH + 64 * wave) * D + 64 * sp) * 2; \
_Pragma("unroll") \
        for (int i = 0; i < 2; ++i) \
          asm volatile("s_mov_b32 %0, m0\n\ts_mov_b32 m0, %4\n\ts_nop 0\n\tbuffer_load_dwordx4 %1, %2, %3 offen nt lds\n\tbuffer_load_dwordx4 %1, %2, %3 offen offset:1152 nt lds\n\t" \
                       "buffer_load_dwordx4 %1, %2, %3 offen offset:2304 nt lds\n\tbuffer_load_dwordx4 %1, %2, %3 offen offset:3456 nt lds\n\ts_mov_b32 m0, %0" \
                       : "=&s"(keep) : "v"(vo), "s"(ro), "s"(so + i * 32 * D * 2), "s"(L_WO + (2 * wave + i) * 4608) : "memory"); \
      } while (0)
      bool wo_now = __builtin_amdgcn_s_memrealtime() - tstart >= WO_AT;
      if (wo_now) WO_DMA();
      BAR();
      f4 d0 = {0, 0, 0, 0}, d1 = d0, d2 = d0, d3 = d0;
#pragma unroll
      for (int s = 0; s < 4; ++s) {
        const bf8 qf = __builtin_bit_cast(bf8, *(LAS v4u*)(L + L_QS + (c & 1) * 256 + (4 * s + g) * 16));
        d0 = __builtin_amdgcn_mfma_f32_16x16x32_bf16(__builtin_bit_cast(bf8, kf[s]), qf, d0, 0, 0, 0);
        d1 = __builtin_amdgcn_mfma_f32_16x16x32_bf16(__builtin_bit_cast(bf8, kf[4 + s]), qf, d1, 0, 0, 0);
        d2 = __builtin_amdgcn_mfma_f32_16x16x32_bf16(__builtin_bit_cast(bf8, kf[8 + s]), qf, d2, 0, 0, 0);
        d3 = __builtin_amdgcn_mfma_f32_16x16x32_bf16(__builtin_bit_cast(bf8, kf[12 + s]), qf, d3, 0, 0, 0);
      }
      const float sc[2] = {pick_score(d0, d1, c), pick_score(d2, d3, c)};
      float m = fmaxf(sc[0], sc[1]); m = fmaxf(m, dpp<X2>(m)); m = fmaxf(m, dpp<R4>(m)); m = fmaxf(m, dpp<R8>(m)); m = mx32(mx16(m));
      if (lane < 2) ((LAS unsigned*)(L + L_WM))[lane * 4 + wave] = nosent(m);
      v4u pa[2]; float pe[2];
#pragma unroll
      for (int G = 0; G < 2; ++G) {
        pe[G] = __builtin_amdgcn_exp2f((sc[G] - m) * 1.4426950408889634f);
        pa[G].x = pk2(pe[G], dpp<0x102>(pe[G])); pa[G].y = dppu<0x104>(pa[G].x); pa[G].z = dppu<0x108>(pa[G].x); pa[G].w = dppu<0x10C>(pa[G].x);
      }
      if (!wo_now && __builtin_amdgcn_s_memrealtime() - tstart >= WO_AT) { wo_now = true; WO_DMA(); }
      f4 o[8];
#pragma unroll
      for (int G = 0; G < 2; ++G) {
#pragma unroll
        for (int e = 0; e < 8; ++e) {
          const v4u bfr = vv[G][e];
          o[e] = __builtin_amdgcn_mfma_f32_16x16x32_bf16(__builtin_bit_cast(bf8, pa[G]), __builtin_bit_cast(bf8, bfr), G ? o[e] : f4{0, 0, 0, 0}, 0, 0, 0);
        }
      }
      float l = pe[0] + pe[1]; l += dpp<X2>(l); l += dpp<R4>(l); l += dpp<R8>(l); l = sw16(l, l); l = sw32(l, l);
      if (lane < 2) ((LAS unsigned*)(L + L_WL))[lane * 4 + wave] = nosent(l);
      if (lane < 16) {
        LAS f4* ow = (LAS f4*)(L + L_OW + wave * 1024 + lane * 32);
        ow[0] = f4{o[0].x, o[1].x, o[2].x, o[3].x}; ow[1] = f4{o[4].x, o[5].x, o[6].x, o[7].x};
        ow[32] = f4{o[0].y, o[1].y, o[2].y, o[3].y}; ow[33] = f4{o[4].y, o[5].y, o[6].y, o[7].y};
      }
      BAR();
      { const v4u tg = *(LAS v4u*)(L + L_TAG); tag.x = __builtin_amdgcn_readfirstlane(tg.x); tag.y = __builtin_amdgcn_readfirstlane(tg.y); nb = __builtin_amdgcn_readfirstlane(tg.z); }
      __builtin_amdgcn_s_barrier();
      if (!wo_now) WO_DMA();
      asm volatile("s_waitcnt vmcnt(0)" ::: "memory");
#pragma unroll
      for (int i = 0; i < 8; ++i) wo8[i] = *(LAS v4u*)(L + L_WO + (2 * wave + (i >> 2)) * 4608 + (i & 3) * 1152 + lane * 16);
      frag8(wo8);
      { v4u pa, pb; v2u ml; o_poll(rs, nb, kvh, wave, lane, pa, pb, ml); o_stage(L, wo8, wave, lane, pa, pb, ml); }
      BAR();
      __builtin_amdgcn_s_barrier();
      mlp_load(p, raw, t); mlp_prep(raw, mw);
    } else {
      __builtin_amdgcn_s_setprio(2);
      if (lane == 0) *(LAS v4u*)(L + L_TAG) = v4u{tag.x, tag.y, (unsigned)nb, 0u};
      st16(rs, j == 16 && lane < 8 ? WS_NB + xc * 128 + lane * 16 : OOB, v4u{(unsigned)nb, tag.x, 0u, tag.y});
      {
        const unsigned char* qa = p.ws + WS_QG + (kvh >> 2) * PR + nb + (kvh & 3) * 2048;
        const unsigned lq = (unsigned)(uintptr_t)(L + L_QP);
#define QSET(K) asm volatile("s_mov_b32 %0, m0\n\ts_mov_b32 m0, %2\n\ts_nop 0\n\tglobal_load_lds_dwordx4 %1, %4 sc1\n\tglobal_load_lds_dwordx4 %1, %4 offset:1024 sc1\n\t" \
                     "s_mov_b32 m0, %3\n\ts_nop 0\n\tglobal_load_lds_dwordx4 %1, %5 sc1\n\tglobal_load_lds_dwordx4 %1, %5 offset:1024 sc1\n\ts_mov_b32 m0, %0" \
                     : "=&s"(keep) : "v"(lane * 16), "s"(lq + (K) * 4096u), "s"(lq + (K) * 4096u + 2048u), "s"(qa), "s"(qa + 4 * PR) : "memory")
        v4u g0, g1, g2, g3;
        __builtin_amdgcn_s_barrier();
        QSET(0);
        asm volatile("s_waitcnt vmcnt(0)" ::: "memory");
        _Pragma("nounroll") for (int s = 0; s < SPIN_CAP; ++s) {
          const LAS v4u* q = (const LAS v4u*)(L + L_QP) + lane;
          g0 = q[0]; g2 = q[64]; g1 = q[128]; g3 = q[192];
          if (!__any(((tag_dif(g0, tag) | tag_dif(g1, tag)) | (tag_dif(g2, tag) | tag_dif(g3, tag))) != 0u)) break;
          QSET(0);
          asm volatile("s_waitcnt vmcnt(0)" ::: "memory");
        }
        ((LAS unsigned*)(L + L_QS))[lane] = pk2(__uint_as_float(g0.x) + __uint_as_float(g1.x), __uint_as_float(g0.z) + __uint_as_float(g1.z));
        ((LAS unsigned*)(L + L_QS))[64 + lane] = pk2(__uint_as_float(g2.x) + __uint_as_float(g3.x), __uint_as_float(g2.z) + __uint_as_float(g3.z));
      }
      if (lane < 16) ((LAS unsigned*)(L + L_WM))[lane] = SENT;
      BAR();
      float mwv;
      _Pragma("nounroll") for (int s = 0; s < (SPIN_CAP << 4); ++s) {
        const unsigned v = *(volatile LAS unsigned*)(L + L_WM + (lane & 7) * 4);
        mwv = __uint_as_float(v);
        if (__all(v != SENT)) break;
        __builtin_amdgcn_s_sleep(1);
      }
      float mh = fmaxf(mwv, dpp<X1>(mwv)); mh = fmaxf(mh, dpp<X2>(mh));
      const float wt = __builtin_amdgcn_exp2f((mwv - mh) * 1.4426950408889634f);
      float w4[4];
#pragma unroll
      for (int w = 0; w < 4; ++w) { const float a0 = rdl(wt, w), a1 = rdl(wt, 4 + w); w4[w] = lane < 32 ? a0 : a1; asm volatile("" : "+v"(w4[w])); }
      {
        float l;
        _Pragma("nounroll") for (int s = 0; s < (SPIN_CAP << 4); ++s) {
          const unsigned v = *(volatile LAS unsigned*)(L + L_WL + (lane & 7) * 4);
          l = wt * __uint_as_float(v);
          if (__all(v != SENT)) break;
        }
        l += dpp<X1>(l); l += dpp<X2>(l);
        const unsigned m_ = nosent(mh), l_ = nosent(l), mo = dppu<X7>(m_), lo = dppu<X7>(l_); const bool h1 = lane & 4;
        st16(rs, lane < 16 ? WS_ML + (kvh >> 3) * PR + (lane >> 3) * BL + (kvh & 7) * 1024 + sp * 128 + (lane & 7) * 16 : OOB, v4u{h1 ? mo : m_, h1 ? lo : l_, h1 ? m_ : mo, h1 ? l_ : lo});
      }
      const int oo = WS_PO + kvh * PR + sp * 1024 + lane * 16;
      BAR();
      const LAS f4* ow = (const LAS f4*)(L + L_OW) + lane;
      const f4 s = (w4[0] * ow[0] + w4[1] * ow[64]) + (w4[2] * ow[128] + w4[3] * ow[192]);
      { const v4u ov = {nosent(s.x), nosent(s.y), nosent(s.z), nosent(s.w)}; st16(rs, oo + nb, ov); st16(rs, oo + (nb ^ BL), ov); }
      __builtin_amdgcn_s_barrier();
      BAR();
      publish_ap(rs, L, kvh, sp, lane);
      __builtin_amdgcn_s_barrier();
    }
  }

  {
    const int d0 = 3 * fi, dl = d0 + (lane & 3), sl = lane >> 3;
    const int offa = WS_XS + nb + ((xc >> 1) * 9 + (lane & 7)) * 128 + sl * 16, dc = dl < D ? dl : d0;
    const int offb = lane < 48 ? WS_XS + nb + ((xc >> 1) * 9 + 8) * 128 + sl * 16 : WS_XG + (dc >> 9) * PR + nb + (dc & 511) * 16;
    v4u xa, xb;
    float x1_d = 0.f;
    if (helper) {
      v4u ya = ld16(rs, offa), yb = ld16(rs, offb), za, zb;
      _Pragma("nounroll") for (int s = 0;; ++s) {
        __builtin_amdgcn_s_sleep(1);
        asm volatile("" ::: "memory");
        za = ld16(rs, offa); zb = ld16(rs, offb);
        PIN();
        if (!__any((tag_dif(ya, tag) | tag_dif(yb, tag)) != 0u) || s >= SPIN_CAP) { xa = ya; xb = yb; break; }
        __builtin_amdgcn_s_sleep(1);
        asm volatile("" ::: "memory");
        ya = ld16(rs, offa); yb = ld16(rs, offb);
        PIN();
        if (!__any((tag_dif(za, tag) | tag_dif(zb, tag)) != 0u)) { xa = za; xb = zb; break; }
      }
      x1_d = bf2f(xb.x);
      const float av_d = __uint_as_float(xb.x & 0xFFFF0000u), sb_d = bf2f(xb.z);
      const float r = sum_lo3(__uint_as_float(xa.x)) + __uint_as_float(xb.x), r0 = sum_lo3(__uint_as_float(xa.z)) + __uint_as_float(xb.z);
      const float sX = rdl(r, 0), sXX = rdl(r, 8), sU = rdl(r, 16), sUU = rdl(r, 24), sUA = rdl(r, 32), sUS = rdl(r, 40);
      const float sA = rdl(r0, 0), sAA = rdl(r0, 8), sAS = rdl(r0, 16), sS = rdl(r0, 24), sSS = rdl(r0, 32);
      const float mean = sX * (1.f / D), var = fmaxf(sXX * (1.f / D) - mean * mean, 0.f);
      const float m1 = rb(mean), r1 = __builtin_amdgcn_rcpf(rb(__builtin_amdgcn_sqrtf(rb(rb(var) + 1e-5f))));
      const float mu = (r1 * (sU - m1 * sA) + sS) * (1.f / D);
      const float e2 = (r1 * r1 * (sUU - 2.f * m1 * sUA + m1 * m1 * sAA) + 2.f * r1 * (sUS - m1 * sAS) + sSS) * (1.f / D);
      const float var2 = fmaxf(e2 - mu * mu, 0.f);
      const float m2 = rb(mu), r2 = __builtin_amdgcn_rcpf(rb(__builtin_amdgcn_sqrtf(rb(rb(var2) + 1e-5f))));
      const float z = rb(rb(x1_d - m1) * r1);
      const float h = rb(rb(z * av_d) + sb_d);
      const float tv = rb(rb(h - m2) * r2);
      if (lane >= 48 && lane < 52) ((LAS float*)(L + L_OWN))[lane - 48] = tv;
    }
    BAR();
    if (!helper) {
      const f4 tv = *(LAS f4*)(L + L_OWN);
      float acc[3];
#pragma unroll
      for (int d = 0; d < 3; ++d) {
        const f2 tt = {tv[d], tv[d]};
        f2 e[9], u[9];
#pragma unroll
        for (int i = 0; i < 9; ++i) e[i] = __builtin_elementwise_fma(mw.A3[i], tt, mw.A2[i]);
        PIN();
#pragma unroll
        for (int i = 0; i < 9; ++i) e[i] = __builtin_elementwise_fma(e[i], tt, mw.A1[i]);
        PIN();
#pragma unroll
        for (int i = 0; i < 9; ++i) e[i] = __builtin_elementwise_fma(e[i], tt, mw.A0[i]);
        PIN();
#pragma unroll
        for (int i = 0; i < 9; ++i) e[i] = f2{__builtin_amdgcn_exp2f(e[i].x), __builtin_amdgcn_exp2f(e[i].y)};
        PIN();
#pragma unroll
        for (int i = 0; i < 9; ++i) e[i] += 1.f;
#pragma unroll
        for (int i = 0; i < 9; ++i) u[i] = __builtin_elementwise_fma(mw.U1[i], tt, mw.U0[i]);
        PIN();
#pragma unroll
        for (int i = 0; i < 9; ++i) e[i] = f2{__builtin_amdgcn_rcpf(e[i].x), __builtin_amdgcn_rcpf(e[i].y)};
        PIN();
        f2 a = u[0] * e[0], a1 = u[1] * e[1], a2 = u[2] * e[2];
#pragma unroll
        for (int i = 3; i < 9; i += 3) { a = __builtin_elementwise_fma(u[i], e[i], a); a1 = __builtin_elementwise_fma(u[i + 1], e[i + 1], a1); a2 = __builtin_elementwise_fma(u[i + 2], e[i + 2], a2); }
        a += a1 + a2;
        PIN();
        acc[d] = a.x + a.y;
      }
      float q = sw16(sw32(acc[0], acc[2]), sw32(acc[1], 0.f));
      q += dpp<R8>(q); q = sum_lo3(q);
      if ((lane & 15) == 0) ((LAS float*)(L + L_FIN))[wave * 4 + (lane >> 4)] = q;
    }
    BAR();
    if (helper) {
      if (lane >= 48 && lane < 51) {
        const LAS float* fin = (const LAS float*)(L + L_FIN) + (lane - 48);
        const float y = rb(rb((fin[0] + fin[4]) + (fin[8] + fin[12])) + bf2f(b2b));
        const u16 res = (u16)(__float_as_uint(rb(x1_d + rb(__uint_as_float(xb.z & 0xFFFF0000u) * y))) >> 16);
        __builtin_amdgcn_raw_buffer_store_b16(res, mkrs(p.out, D * 2), dl * 2, 0, 0);
      }
      if (fi == 0 && lane == 0) st8(rs, WS_EP, tag);
    }
  }
}

extern "C" void kernel_launch(void* const* d_in, const int* in_sizes, int n_in, void* d_out, int out_size, void* d_ws, size_t ws_size, hipStream_t stream) {
  Params p;
  p.x = (const u16*)d_in[0]; p.kc = (const u16*)d_in[1]; p.vc = (const u16*)d_in[2]; p.wq = (const u16*)d_in[3]; p.wo = (const u16*)d_in[4];
  p.w1 = (const u16*)d_in[6]; p.b1 = (const u16*)d_in[7]; p.w2 = (const u16*)d_in[8]; p.b2 = (const u16*)d_in[9];
  p.c = (const u16*)d_in[10]; p.wm = (const u16*)d_in[11];
  p.out = (u16*)d_out; p.ws = (unsigned char*)d_ws;
  fused_k<<<NWG, NTHR, 0, stream>>>(p);
}
```
